```python
import torch

def _softmax(x, axis=-1):
    return torch.softmax(x, dim=axis)
def _gelu(x):
    return torch.nn.functional.gelu(x, approximate="tanh")
def _silu(x):
    return torch.nn.functional.silu(x)
def _sigmoid(x):
    return torch.sigmoid(x)
def _layernorm(x, axis=-1, eps=1e-5):
    m = x.mean(dim=axis, keepdim=True)
    v = x.var(dim=axis, keepdim=True, unbiased=False)
    return (x - m) / torch.sqrt(v + eps)
def _scatter_add(tgt, idx, src, axis=0):
    return tgt.clone().index_add_(axis, idx, src)
def _segment_sum(src, seg, n):
    out = torch.zeros((n,) + src.shape[1:], dtype=src.dtype)
    return out.index_add_(0, seg, src)
def _pad(x, pw):
    flat = tuple(v for pair in reversed(pw) for v in pair)
    return torch.nn.functional.pad(x, flat)

kernel_name = 'standalone__sliding_block_cached__decode'

def setup_inputs():

    _w = (lambda a: (lambda t: t.to(torch.bfloat16) if t.is_floating_point() else t)(torch.as_tensor(a)))
    B, Sq, D = 1, 1, 640
    Hq, Hkv, Sk, Dh = 32, 32, 4096, 192
    assert Hq % Hkv == 0, (Hq, Hkv)
    return {
        "x": _w((lambda *s: torch.randn(*s, dtype=torch.bfloat16) * 1.6)(B, Sq, D)),
        "k_cache": _w((lambda *s: torch.randn(*s, dtype=torch.bfloat16) * 1.6)(B, Hkv, Sk, Dh)),
        "v_cache": _w((lambda *s: torch.randn(*s, dtype=torch.bfloat16) * 0.05)(B, Hkv, Sk, Dh)),
        "wq": _w((lambda *s: torch.randn(*s, dtype=torch.bfloat16) * 1.6)(D, Hq * Dh) * (D ** -0.5)),
        "wo": _w((lambda *s: torch.randn(*s, dtype=torch.bfloat16) * 0.05)(Hq * Dh, D) * ((Hq * Dh) ** -0.5)),
    }

def reference(x, k_cache, v_cache, wq, wo):
    B, Sq, D = x.shape
    Hq, Dh = 32, 192
    Hkv, Sk = k_cache.shape[1], k_cache.shape[2]
    W = min(4096, max(256, Sk // 4))
    rep = Hq // Hkv
    q = (lambda x, *d: x.permute(*d))((x @ wq).reshape(B, Sq, Hq, Dh), 0, 2, 1, 3)
    k = (lambda x, n, axis: torch.repeat_interleave(x, n, dim=axis))(k_cache[:, :, Sk - W:, :], rep, axis=1)
    v = (lambda x, n, axis: torch.repeat_interleave(x, n, dim=axis))(v_cache[:, :, Sk - W:, :], rep, axis=1)
    scores = torch.einsum("bhqd,bhkd->bhqk", q, k) * (Dh ** -0.5)
    p = _softmax(scores, axis=-1)
    o = torch.einsum("bhqk,bhkd->bhqd", p, v)
    return (lambda x, *d: x.permute(*d))(o, 0, 2, 1, 3).reshape(B, Sq, Hq * Dh) @ wo
```

```cpp
#include <hip/hip_runtime.h>
#include <hip/hip_bf16.h>
#include <stdint.h>

constexpr int D = 640, HQ = 32, DH = 192, SK = 4096, W = 1024, NS = 8, KPS = W / NS;
typedef unsigned v4u __attribute__((ext_vector_type(4)));
typedef __attribute__((ext_vector_type(2))) __bf16 bf16x2;

__device__ __forceinline__ float bflo(unsigned u) { return __uint_as_float(u << 16); }
__device__ __forceinline__ float bfhi(unsigned u) { return __uint_as_float(u & 0xffff0000u); }
__device__ __forceinline__ float rbf(float f) {
  unsigned u = __float_as_uint(f);
  u = (u + 0x7fffu + ((u >> 16) & 1u)) & 0xffff0000u;
  return __uint_as_float(u);
}
__device__ __forceinline__ float dot2(unsigned k, unsigned q, float acc) {
  return __builtin_amdgcn_fdot2_f32_bf16(__builtin_bit_cast(bf16x2, k), __builtin_bit_cast(bf16x2, q), acc, false);
}

__global__ void __launch_bounds__(512) k_qproj(const unsigned short* x, const unsigned short* wq, float* qpart) {
  __shared__ float red[8][192];
  const int h = blockIdx.x >> 3, s = blockIdx.x & 7;
  const int tid = threadIdx.x, lane = tid & 63, wave = tid >> 6, rp = lane >> 3, j = lane & 7;
  float acc[3][8];
  for (int i = 0; i < 3; ++i) for (int e = 0; e < 8; ++e) acc[i][e] = 0.f;
  for (int g = wave; g < 10; g += 8) {
    const int row = 80 * s + 8 * g + rp;
    const float xv = __uint_as_float((unsigned)x[row] << 16);
    for (int i = 0; i < 3; ++i) {
      v4u w = *(const v4u*)(wq + (size_t)row * (HQ * DH) + h * DH + 64 * i + 8 * j);
      for (int e = 0; e < 4; ++e) { acc[i][2 * e] += xv * bflo(w[e]); acc[i][2 * e + 1] += xv * bfhi(w[e]); }
    }
  }
  for (int i = 0; i < 3; ++i) for (int e = 0; e < 8; ++e) {
    float v = acc[i][e];
    v += __shfl_xor(v, 8); v += __shfl_xor(v, 16); v += __shfl_xor(v, 32);
    acc[i][e] = v;
  }
  if (rp == 0) for (int i = 0; i < 3; ++i) for (int e = 0; e < 8; ++e) red[wave][64 * i + 8 * j + e] = acc[i][e];
  __syncthreads();
  if (tid < 192) { float v = 0.f; for (int w = 0; w < 8; ++w) v += red[w][tid]; qpart[(h * 8 + s) * 192 + tid] = v; }
}

__global__ void __launch_bounds__(512) k_attn(const unsigned short* kc, const unsigned short* vc, const float* qpart, float* opart) {
  __shared__ float qs[192];
  __shared__ float sc[128];
  __shared__ float red[8][192];
  __shared__ float wm[8];
  const int h = blockIdx.x >> 3, s = blockIdx.x & 7;
  const int tid = threadIdx.x, lane = tid & 63, wave = tid >> 6, rp = lane >> 3, j = lane & 7;
  if (tid < 192) { float v = 0.f; for (int p = 0; p < 8; ++p) v += qpart[(h * 8 + p) * 192 + tid]; qs[tid] = rbf(v); }
  __syncthreads();
  unsigned qp[3][4];
  for (int i = 0; i < 3; ++i) for (int e = 0; e < 4; ++e)
    qp[i][e] = (__float_as_uint(qs[64 * i + 8 * j + 2 * e]) >> 16) | (__float_as_uint(qs[64 * i + 8 * j + 2 * e + 1]) & 0xffff0000u);
  const size_t base = ((size_t)h * SK + (SK - W) + KPS * s) * DH;
  float sv[2];
  for (int g = 0; g < 2; ++g) {
    const int row = 16 * wave + 8 * g + rp;
    float d = 0.f;
    for (int i = 0; i < 3; ++i) {
      v4u k = *(const v4u*)(kc + base + (size_t)row * DH + 64 * i + 8 * j);
      for (int e = 0; e < 4; ++e) d = dot2(k[e], qp[i][e], d);
    }
    d += __shfl_xor(d, 1); d += __shfl_xor(d, 2); d += __shfl_xor(d, 4);
    sv[g] = rbf(rbf(d) * 0.07216878364870322f);
  }
  float m = fmaxf(sv[0], sv[1]);
  m = fmaxf(m, __shfl_xor(m, 8)); m = fmaxf(m, __shfl_xor(m, 16)); m = fmaxf(m, __shfl_xor(m, 32));
  if (lane == 0) wm[wave] = m;
  __syncthreads();
  float M = wm[0];
  for (int w = 1; w < 8; ++w) M = fmaxf(M, wm[w]);
  float acc[3][8];
  for (int i = 0; i < 3; ++i) for (int e = 0; e < 8; ++e) acc[i][e] = 0.f;
  float l = 0.f;
  for (int g = 0; g < 2; ++g) {
    const int row = 16 * wave + 8 * g + rp;
    const float p = __expf(sv[g] - M);
    l += p;
    for (int i = 0; i < 3; ++i) {
      v4u v = *(const v4u*)(vc + base + (size_t)row * DH + 64 * i + 8 * j);
      for (int e = 0; e < 4; ++e) { acc[i][2 * e] += p * bflo(v[e]); acc[i][2 * e + 1] += p * bfhi(v[e]); }
    }
  }
  l += __shfl_xor(l, 8); l += __shfl_xor(l, 16); l += __shfl_xor(l, 32);
  for (int i = 0; i < 3; ++i) for (int e = 0; e < 8; ++e) {
    float v = acc[i][e];
    v += __shfl_xor(v, 8); v += __shfl_xor(v, 16); v += __shfl_xor(v, 32);
    acc[i][e] = v;
  }
  __syncthreads();
  if (rp == 0) for (int i = 0; i < 3; ++i) for (int e = 0; e < 8; ++e) red[wave][64 * i + 8 * j + e] = acc[i][e];
  if (lane == 0) wm[wave] = l;
  __syncthreads();
  float* op = opart + (h * 8 + s) * 256;
  if (tid < 192) { float v = 0.f; for (int w = 0; w < 8; ++w) v += red[w][tid]; op[tid] = v; }
  if (tid == 192) { float v = 0.f; for (int w = 0; w < 8; ++w) v += wm[w]; op[192] = M; op[193] = v; }
}

__global__ void __launch_bounds__(512) k_oproj(const unsigned short* wo, const float* opart, float* outpart) {
  __shared__ float os[24];
  __shared__ float red[320 * 8];
  const int h = blockIdx.x >> 3, s = blockIdx.x & 7;
  const int tid = threadIdx.x, lane = tid & 63, wave = tid >> 6;
  if (tid < 24) {
    float M = -INFINITY;
    for (int p = 0; p < 8; ++p) M = fmaxf(M, opart[(h * 8 + p) * 256 + 192]);
    float L = 0.f, o = 0.f;
    for (int p = 0; p < 8; ++p) {
      const float c = __expf(opart[(h * 8 + p) * 256 + 192] - M);
      L += c * opart[(h * 8 + p) * 256 + 193];
      o += c * opart[(h * 8 + p) * 256 + 24 * s + tid];
    }
    os[tid] = rbf(o / L);
  }
  __syncthreads();
  if (wave < 5) {
    const int c = 64 * wave + lane, ro = c / 80, col = c % 80;
    float acc[8];
    for (int e = 0; e < 8; ++e) acc[e] = 0.f;
    const unsigned short* wb = wo + (size_t)(h * DH + 24 * s) * D;
    for (int b = 0; b < 6; ++b) {
      v4u w = *(const v4u*)(wb + (size_t)(4 * b) * D + 8 * c);
      const float ov = os[4 * b + ro];
      for (int e = 0; e < 4; ++e) { acc[2 * e] += ov * bflo(w[e]); acc[2 * e + 1] += ov * bfhi(w[e]); }
    }
    for (int e = 0; e < 8; ++e) red[c * 8 + e] = acc[e];
  }
  __syncthreads();
  for (int t = tid; t < 640; t += 512) {
    float v = red[t] + red[640 + t] + red[1280 + t] + red[1920 + t];
    outpart[blockIdx.x * 640 + t] = v;
  }
}

__global__ void __launch_bounds__(256) k_final(const float* outpart, unsigned short* out) {
  __shared__ float red[4][64];
  const int col = blockIdx.x * 64 + (threadIdx.x & 63), wave = threadIdx.x >> 6;
  float v = 0.f;
  for (int p = wave * 64; p < wave * 64 + 64; ++p) v += outpart[p * 640 + col];
  red[wave][threadIdx.x & 63] = v;
  __syncthreads();
  if (wave == 0) {
    float t = red[0][threadIdx.x] + red[1][threadIdx.x] + red[2][threadIdx.x] + red[3][threadIdx.x];
    out[col] = (unsigned short)(__float_as_uint(rbf(t)) >> 16);
  }
}

extern "C" void kernel_launch(void* const* d_in, const int* in_sizes, int n_in,
                              void* d_out, int out_size, void* d_ws, size_t ws_size,
                              hipStream_t stream) {
  const unsigned short* x = (const unsigned short*)d_in[0];
  const unsigned short* kc = (const unsigned short*)d_in[1];
  const unsigned short* vc = (const unsigned short*)d_in[2];
  const unsigned short* wq = (const unsigned short*)d_in[3];
  const unsigned short* wo = (const unsigned short*)d_in[4];
  float* ws = (float*)d_ws;
  float* qpart = ws;
  float* opart = ws + 65536;
  float* outpart = ws + 131072;
  k_qproj<<<256, 512, 0, stream>>>(x, wq, qpart);
  k_attn<<<256, 512, 0, stream>>>(kc, vc, qpart, opart);
  k_oproj<<<256, 512, 0, stream>>>(wo, opart, outpart);
  k_final<<<10, 256, 0, stream>>>(outpart, (unsigned short*)d_out);
}
```

```cpp
#include <hip/hip_runtime.h>
#include <hip/hip_bf16.h>
#include <stdint.h>

constexpr int D = 640, HQ = 32, DH = 192, SK = 4096, W = 1024, NS = 8, KPS = W / NS;
constexpr int NWS = 10;
constexpr int NT = 64 * (NWS + 1);
constexpr int NRED = 40;

typedef unsigned v4u __attribute__((ext_vector_type(4)));
typedef __attribute__((ext_vector_type(2))) __bf16 bf16x2;
typedef unsigned short u16;

constexpr unsigned WS_E = 0;
constexpr unsigned WS_QX = 4096;
constexpr unsigned WS_OX = WS_QX + 256 * 96 * 16;
constexpr unsigned WS_PX = WS_OX + 32 * 64 * 256;
constexpr unsigned WS_END = WS_PX + 256 * 320 * 16;
constexpr unsigned SPIN_CAP = 1u << 16;

__device__ __forceinline__ float bflo(unsigned u) { return __uint_as_float(u << 16); }
__device__ __forceinline__ float bfhi(unsigned u) { return __uint_as_float(u & 0xffff0000u); }
__device__ __forceinline__ float rbf(float f) {
  unsigned u = __float_as_uint(f);
  u = (u + 0x7fffu + ((u >> 16) & 1u)) & 0xffff0000u;
  return __uint_as_float(u);
}
__device__ __forceinline__ float dot2(unsigned k, unsigned q, float acc) {
  return __builtin_amdgcn_fdot2_f32_bf16(__builtin_bit_cast(bf16x2, k), __builtin_bit_cast(bf16x2, q), acc, false);
}
__device__ __forceinline__ v4u ldnt(const u16* p) { return __builtin_nontemporal_load((const v4u*)p); }
__device__ __forceinline__ unsigned rotl(unsigned v, int r) { return (v << r) | (v >> (32 - r)); }
__device__ __forceinline__ v4u mkgran(float a, float b, unsigned A, unsigned B) {
  const unsigned v0 = __float_as_uint(a), v1 = __float_as_uint(b);
  v4u g; g[0] = v0; g[1] = v1; g[2] = A ^ v0 ^ rotl(v1, 16); g[3] = B ^ v1 ^ rotl(v0, 9);
  return g;
}
__device__ __forceinline__ bool okgran(v4u g, unsigned A, unsigned B) {
  return (g[2] == (A ^ g[0] ^ rotl(g[1], 16))) & (g[3] == (B ^ g[1] ^ rotl(g[0], 9)));
}

template <bool HAS_KV>
__device__ __forceinline__ void stream_role(const u16* __restrict__ x, const u16* __restrict__ kc, const u16* __restrict__ vc,
    const u16* __restrict__ wq, const u16* __restrict__ wo, int h, int s, int wave, int lane,
    float (*red)[192], float* red2, const unsigned* qs, const float* os, float (*wml)[2]) {
  const int rp = lane >> 3, j = lane & 7;
    const int qrow = 80 * s + 8 * wave + rp;
    const unsigned xr = x[qrow];
    v4u WQ[3], KK[2][3], VV[2][3], WO[3];
#pragma unroll
    for (int i = 0; i < 3; ++i) WQ[i] = ldnt(wq + (size_t)qrow * (HQ * DH) + h * DH + 64 * i + 8 * j);
    const size_t kvbase = ((size_t)h * SK + (SK - W) + KPS * s + 16 * wave + rp) * DH + 8 * j;
    if constexpr (HAS_KV) {
#pragma unroll
      for (int g = 0; g < 2; ++g)
#pragma unroll
        for (int i = 0; i < 3; ++i) KK[g][i] = ldnt(kc + kvbase + (size_t)g * 8 * DH + 64 * i);
#pragma unroll
      for (int g = 0; g < 2; ++g)
#pragma unroll
        for (int i = 0; i < 3; ++i) VV[g][i] = ldnt(vc + kvbase + (size_t)g * 8 * DH + 64 * i);
    }
    const int cls = wave % 5, half = wave / 5;
    const int oc = 64 * cls + lane, oro = oc / 80;
    {
      const u16* wb = wo + (size_t)(h * DH + 24 * s + 12 * half) * D + 8 * oc;
#pragma unroll
      for (int i = 0; i < 3; ++i) WO[i] = ldnt(wb + (size_t)(4 * i) * D);
    }
    __builtin_amdgcn_sched_barrier(0);
    {
      const float xv = __uint_as_float(xr << 16);
      float a[3][8];
#pragma unroll
      for (int i = 0; i < 3; ++i)
#pragma unroll
        for (int e = 0; e < 4; ++e) { a[i][2 * e] = xv * bflo(WQ[i][e]); a[i][2 * e + 1] = xv * bfhi(WQ[i][e]); }
#pragma unroll
      for (int i = 0; i < 3; ++i)
#pragma unroll
        for (int e = 0; e < 8; ++e) {
          float v = a[i][e];
          v += __shfl_xor(v, 8); v += __shfl_xor(v, 16); v += __shfl_xor(v, 32);
          a[i][e] = v;
        }
      if (rp == 0) {
#pragma unroll
        for (int i = 0; i < 3; ++i) {
          *(float4*)&red[wave][64 * i + 8 * j] = make_float4(a[i][0], a[i][1], a[i][2], a[i][3]);
          *(float4*)&red[wave][64 * i + 8 * j + 4] = make_float4(a[i][4], a[i][5], a[i][6], a[i][7]);
        }
      }
    }
    __syncthreads();
    __syncthreads();
    if constexpr (HAS_KV) {
      v4u qp[3];
#pragma unroll
      for (int i = 0; i < 3; ++i) qp[i] = *(const v4u*)&qs[32 * i + 4 * j];
      float sv[2];
#pragma unroll
      for (int g = 0; g < 2; ++g) {
        float d = 0.f;
#pragma unroll
        for (int i = 0; i < 3; ++i)
#pragma unroll
          for (int e = 0; e < 4; ++e) d = dot2(KK[g][i][e], qp[i][e], d);
        d += __shfl_xor(d, 1); d += __shfl_xor(d, 2); d += __shfl_xor(d, 4);
        sv[g] = rbf(rbf(d) * 0.07216878364870322f);
      }
      float m = fmaxf(sv[0], sv[1]);
      m = fmaxf(m, __shfl_xor(m, 8)); m = fmaxf(m, __shfl_xor(m, 16)); m = fmaxf(m, __shfl_xor(m, 32));
      const float p0 = __expf(sv[0] - m), p1 = __expf(sv[1] - m);
      float l = p0 + p1;
      l += __shfl_xor(l, 8); l += __shfl_xor(l, 16); l += __shfl_xor(l, 32);
      float a[3][8];
#pragma unroll
      for (int i = 0; i < 3; ++i)
#pragma unroll
        for (int e = 0; e < 4; ++e) {
          a[i][2 * e] = p0 * bflo(VV[0][i][e]) + p1 * bflo(VV[1][i][e]);
          a[i][2 * e + 1] = p0 * bfhi(VV[0][i][e]) + p1 * bfhi(VV[1][i][e]);
        }
#pragma unroll
      for (int i = 0; i < 3; ++i)
#pragma unroll
        for (int e = 0; e < 8; ++e) {
          float v = a[i][e];
          v += __shfl_xor(v, 8); v += __shfl_xor(v, 16); v += __shfl_xor(v, 32);
          a[i][e] = v;
        }
      if (rp == 0) {
#pragma unroll
        for (int i = 0; i < 3; ++i) {
          *(float4*)&red[wave][64 * i + 8 * j] = make_float4(a[i][0], a[i][1], a[i][2], a[i][3]);
          *(float4*)&red[wave][64 * i + 8 * j + 4] = make_float4(a[i][4], a[i][5], a[i][6], a[i][7]);
        }
      }
      if (lane == 0) { wml[wave][0] = m; wml[wave][1] = l; }
    }
    __syncthreads();
    __syncthreads();
    {
      float a[8];
#pragma unroll
      for (int e = 0; e < 8; ++e) a[e] = 0.f;
#pragma unroll
      for (int i = 0; i < 3; ++i) {
        const float ov = os[12 * half + 4 * i + oro];
#pragma unroll
        for (int e = 0; e < 4; ++e) { a[2 * e] += ov * bflo(WO[i][e]); a[2 * e + 1] += ov * bfhi(WO[i][e]); }
      }
      *(float4*)&red2[(wave * 64 + lane) * 8] = make_float4(a[0], a[1], a[2], a[3]);
      *(float4*)&red2[(wave * 64 + lane) * 8 + 4] = make_float4(a[4], a[5], a[6], a[7]);
    }
}

__global__ void __launch_bounds__(NT, 1)
fused_decode(const u16* __restrict__ x, const u16* __restrict__ kc, const u16* __restrict__ vc,
             const u16* __restrict__ wq, const u16* __restrict__ wo, u16* __restrict__ out, unsigned char* ws) {
  __shared__ __attribute__((aligned(16))) float red[NWS][192];
  __shared__ __attribute__((aligned(16))) float red2[NWS * 64 * 8];
  __shared__ __attribute__((aligned(16))) unsigned qs[96];
  __shared__ __attribute__((aligned(16))) float os[24];
  __shared__ float wml[8][2];
  __shared__ unsigned tag[2];
  __shared__ float fin[4][16];

  const int b = blockIdx.x;
  const int h = (b & 7) * 4 + ((b >> 3) & 3), s = b >> 5;
  const int tid = threadIdx.x, lane = tid & 63;
  const int wave = __builtin_amdgcn_readfirstlane(tid >> 6);
  const int rp = lane >> 3, j = lane & 7;
  const __amdgpu_buffer_rsrc_t rs = __builtin_amdgcn_make_buffer_rsrc(ws, 0, WS_END, 0x00020000);

  if (wave < 8) {
    stream_role<true>(x, kc, vc, wq, wo, h, s, wave, lane, red, red2, qs, os, wml);
  } else if (wave < NWS) {
    stream_role<false>(x, kc, vc, wq, wo, h, s, wave, lane, red, red2, qs, os, wml);
  } else {
    const unsigned long long E = __hip_atomic_load((unsigned long long*)(ws + WS_E), __ATOMIC_RELAXED, __HIP_MEMORY_SCOPE_AGENT);
    const unsigned long long T = E + 1ull;
    const unsigned A = (unsigned)T, B = (unsigned)(T >> 32) ^ ((unsigned)T * 0x9E3779B9u);
    if (lane == 0) { tag[0] = A; tag[1] = B; }
    __syncthreads();
    {
      float s0 = 0.f, s1 = 0.f, t0 = 0.f, t1 = 0.f;
#pragma unroll
      for (int w = 0; w < NWS; ++w) {
        const float2 u = *(const float2*)&red[w][2 * lane];
        s0 += u.x; s1 += u.y;
        const float2 v = *(const float2*)&red[w][128 + 2 * (lane & 31)];
        t0 += v.x; t1 += v.y;
      }
      const unsigned off = WS_QX + (unsigned)(h * 8 + s) * 1536u;
      __builtin_amdgcn_raw_buffer_store_b128(mkgran(s0, s1, A, B), rs, off + lane * 16, 0, 16);
      if (lane < 32) __builtin_amdgcn_raw_buffer_store_b128(mkgran(t0, t1, A, B), rs, off + 1024 + lane * 16, 0, 16);
    }
    {
      v4u g0[8], g1[8];
      for (unsigned spin = 0; spin < SPIN_CAP; ++spin) {
        asm volatile("" ::: "memory");
        bool ok = true;
#pragma unroll
        for (int p = 0; p < 8; ++p) {
          const unsigned off = WS_QX + (unsigned)(h * 8 + p) * 1536u;
          g0[p] = __builtin_amdgcn_raw_buffer_load_b128(rs, off + lane * 16, 0, 16);
          g1[p] = __builtin_amdgcn_raw_buffer_load_b128(rs, off + 1024 + (lane & 31) * 16, 0, 16);
        }
#pragma unroll
        for (int p = 0; p < 8; ++p) ok = ok & okgran(g0[p], A, B) & okgran(g1[p], A, B);
        if (__all(ok)) break;
        __builtin_amdgcn_s_sleep(1);
      }
      float s0 = 0.f, s1 = 0.f, t0 = 0.f, t1 = 0.f;
#pragma unroll
      for (int p = 0; p < 8; ++p) {
        s0 += __uint_as_float(g0[p][0]); s1 += __uint_as_float(g0[p][1]);
        t0 += __uint_as_float(g1[p][0]); t1 += __uint_as_float(g1[p][1]);
      }
      qs[lane] = (__float_as_uint(rbf(s0)) >> 16) | __float_as_uint(rbf(s1));
      if (lane < 32) qs[64 + lane] = (__float_as_uint(rbf(t0)) >> 16) | __float_as_uint(rbf(t1));
    }
    __syncthreads();
    __syncthreads();
    {
      float M = wml[0][0];
#pragma unroll
      for (int w = 1; w < 8; ++w) M = fmaxf(M, wml[w][0]);
      float c[8], L = 0.f;
#pragma unroll
      for (int w = 0; w < 8; ++w) { c[w] = __expf(wml[w][0] - M); L += c[w] * wml[w][1]; }
#pragma unroll
      for (int k = 0; k < 2; ++k) {
        const int cs = (lane >> 4) + 4 * k, gi = lane & 15;
        const int pr = 12 * cs + (gi < 12 ? gi : 0);
        float o0 = 0.f, o1 = 0.f;
#pragma unroll
        for (int w = 0; w < 8; ++w) { const float2 u = *(const float2*)&red[w][2 * pr]; o0 += c[w] * u.x; o1 += c[w] * u.y; }
        if (gi == 12) { o0 = M; o1 = L; }
        const unsigned off = WS_OX + (unsigned)((h * 8 + cs) * 8 + s) * 256u + gi * 16;
        if (gi < 13) __builtin_amdgcn_raw_buffer_store_b128(mkgran(o0, o1, A, B), rs, off, 0, 16);
      }
    }
    {
      v4u g[2];
      const int gi = lane & 15;
      const unsigned off = WS_OX + (unsigned)((h * 8 + s) * 8) * 256u + (gi < 13 ? lane : (lane & 48)) * 16;
      for (unsigned spin = 0; spin < SPIN_CAP; ++spin) {
        asm volatile("" ::: "memory");
        g[0] = __builtin_amdgcn_raw_buffer_load_b128(rs, off, 0, 16);
        g[1] = __builtin_amdgcn_raw_buffer_load_b128(rs, off + 1024, 0, 16);
        const bool ok = okgran(g[0], A, B) & okgran(g[1], A, B);
        if (__all(ok)) break;
        __builtin_amdgcn_s_sleep(1);
      }
      const int src = (lane & 48) + 12;
      const float m0 = __shfl(__uint_as_float(g[0][0]), src), l0 = __shfl(__uint_as_float(g[0][1]), src);
      const float m1 = __shfl(__uint_as_float(g[1][0]), src), l1 = __shfl(__uint_as_float(g[1][1]), src);
      float M = fmaxf(m0, m1);
      M = fmaxf(M, __shfl_xor(M, 16)); M = fmaxf(M, __shfl_xor(M, 32));
      const float c0 = __expf(m0 - M), c1 = __expf(m1 - M);
      float L = c0 * l0 + c1 * l1;
      float o0 = c0 * __uint_as_float(g[0][0]) + c1 * __uint_as_float(g[1][0]);
      float o1 = c0 * __uint_as_float(g[0][1]) + c1 * __uint_as_float(g[1][1]);
      L += __shfl_xor(L, 16); L += __shfl_xor(L, 32);
      o0 += __shfl_xor(o0, 16); o0 += __shfl_xor(o0, 32);
      o1 += __shfl_xor(o1, 16); o1 += __shfl_xor(o1, 32);
      if (lane < 12) { os[2 * lane] = rbf(o0 / L); os[2 * lane + 1] = rbf(o1 / L); }
    }
    __syncthreads();
  }
  __syncthreads();
  const unsigned A = tag[0], B = tag[1];
  if (tid < 320) {
    float o0 = 0.f, o1 = 0.f;
#pragma unroll
    for (int q = 0; q < 8; ++q) {
      const float2 u = *(const float2*)&red2[(q >> 2) * 2560 + (q & 3) * 640 + 2 * tid];
      o0 += u.x; o1 += u.y;
    }
    __builtin_amdgcn_raw_buffer_store_b128(mkgran(o0, o1, A, B), rs, WS_PX + (unsigned)b * 5120u + tid * 16, 0, 16);
  }
  if (b < NRED) {
    if (tid < 256) {
      v4u g[8];
      const unsigned off = WS_PX + (unsigned)tid * 5120u + (unsigned)b * 128u;
      for (unsigned spin = 0; spin < SPIN_CAP; ++spin) {
        asm volatile("" ::: "memory");
        bool ok = true;
#pragma unroll
        for (int k = 0; k < 8; ++k) g[k] = __builtin_amdgcn_raw_buffer_load_b128(rs, off + 16 * k, 0, 16);
#pragma unroll
        for (int k = 0; k < 8; ++k) ok = ok & okgran(g[k], A, B);
        if (__all(ok)) break;
        __builtin_amdgcn_s_sleep(1);
      }
      float v[16];
#pragma unroll
      for (int k = 0; k < 8; ++k) { v[2 * k] = __uint_as_float(g[k][0]); v[2 * k + 1] = __uint_as_float(g[k][1]); }
#pragma unroll
      for (int k = 0; k < 8; ++k) { const bool up = lane & 32; const float keep = up ? v[k + 8] : v[k], send = up ? v[k] : v[k + 8]; v[k] = keep + __shfl_xor(send, 32); }
#pragma unroll
      for (int k = 0; k < 4; ++k) { const bool up = lane & 16; const float keep = up ? v[k + 4] : v[k], send = up ? v[k] : v[k + 4]; v[k] = keep + __shfl_xor(send, 16); }
#pragma unroll
      for (int k = 0; k < 2; ++k) { const bool up = lane & 8; const float keep = up ? v[k + 2] : v[k], send = up ? v[k] : v[k + 2]; v[k] = keep + __shfl_xor(send, 8); }
      { const bool up = lane & 4; const float keep = up ? v[1] : v[0], send = up ? v[0] : v[1]; v[0] = keep + __shfl_xor(send, 4); }
      v[0] += __shfl_xor(v[0], 2); v[0] += __shfl_xor(v[0], 1);
      const int col = ((lane >> 5) & 1) * 8 + ((lane >> 4) & 1) * 4 + ((lane >> 3) & 1) * 2 + ((lane >> 2) & 1);
      if ((lane & 3) == 0) fin[tid >> 6][col] = v[0];
    }
    __syncthreads();
    if (tid < 16) {
      const float t = (fin[0][tid] + fin[1][tid]) + (fin[2][tid] + fin[3][tid]);
      out[b * 16 + tid] = (u16)(__float_as_uint(rbf(t)) >> 16);
    }
    if (b == 0 && tid == 64) {
      const unsigned long long T = ((unsigned long long)(B ^ (A * 0x9E3779B9u)) << 32) | A;
      __hip_atomic_store((unsigned long long*)(ws + WS_E), T, __ATOMIC_RELAXED, __HIP_MEMORY_SCOPE_AGENT);
    }
  }
}

extern "C" void kernel_launch(void* const* d_in, const int* in_sizes, int n_in,
                              void* d_out, int out_size, void* d_ws, size_t ws_size,
                              hipStream_t stream) {
  fused_decode<<<256, NT, 0, stream>>>((const u16*)d_in[0], (const u16*)d_in[1], (const u16*)d_in[2],
                                       (const u16*)d_in[3], (const u16*)d_in[4], (u16*)d_out, (unsigned char*)d_ws);
}
```

```cpp
#include <hip/hip_runtime.h>
#include <hip/hip_bf16.h>
#include <stdint.h>

constexpr int D = 640, HQ = 32, DH = 192, SK = 4096, W = 1024, NS = 8, KPS = W / NS;
constexpr int NWS = 10;
constexpr int NT = 64 * (NWS + 1);
constexpr int NRED = 40;

typedef unsigned v4u __attribute__((ext_vector_type(4)));
typedef __attribute__((ext_vector_type(2))) __bf16 bf16x2;
typedef unsigned short u16;

constexpr unsigned WS_E = 0;
constexpr unsigned WS_QX = 4096;
constexpr unsigned WS_OX = WS_QX + 256 * 2048;
constexpr unsigned WS_PX = WS_OX + 256 * 2048;
constexpr unsigned WS_END = WS_PX + NRED * 4096;
#define WIN 2
constexpr unsigned SPIN_CAP = 1u << 16;
#define ST(k)

__device__ __forceinline__ float bflo(unsigned u) { return __uint_as_float(u << 16); }
__device__ __forceinline__ float bfhi(unsigned u) { return __uint_as_float(u & 0xffff0000u); }
__device__ __forceinline__ float rbf(float f) {
  unsigned u = __float_as_uint(f);
  u = (u + 0x7fffu + ((u >> 16) & 1u)) & 0xffff0000u;
  return __uint_as_float(u);
}
__device__ __forceinline__ float dot2(unsigned k, unsigned q, float acc) {
  return __builtin_amdgcn_fdot2_f32_bf16(__builtin_bit_cast(bf16x2, k), __builtin_bit_cast(bf16x2, q), acc, false);
}
__device__ __forceinline__ v4u ldnt(const u16* p) { return __builtin_nontemporal_load((const v4u*)p); }
__device__ __forceinline__ unsigned rotl(unsigned v, int r) { return (v << r) | (v >> (32 - r)); }
__device__ __forceinline__ v4u mkgran(float a, float b, unsigned A, unsigned B) {
  const unsigned v0 = __float_as_uint(a), v1 = __float_as_uint(b);
  v4u g; g[0] = v0; g[1] = v1; g[2] = A ^ v0 ^ rotl(v1, 16); g[3] = B ^ v1 ^ rotl(v0, 9);
  return g;
}
__device__ __forceinline__ bool okgran(v4u g, unsigned A, unsigned B) {
  return (g[2] == (A ^ g[0] ^ rotl(g[1], 16))) & (g[3] == (B ^ g[1] ^ rotl(g[0], 9)));
}
template <int CTRL> __device__ __forceinline__ float dpp(float x) {
  return __builtin_bit_cast(float, __builtin_amdgcn_mov_dpp(__builtin_bit_cast(int, x), CTRL, 0xf, 0xf, true));
}
constexpr int XOR1 = 0xB1, XOR2 = 0x4E, XOR7 = 0x141, XOR8 = 0x128;
__device__ __forceinline__ float tr32(float a, float b) {
  auto s = __builtin_amdgcn_permlane32_swap(__float_as_uint(a), __float_as_uint(b), false, false);
  return __uint_as_float(s[0]) + __uint_as_float(s[1]);
}
__device__ __forceinline__ float tr16(float a, float b) {
  auto s = __builtin_amdgcn_permlane16_swap(__float_as_uint(a), __float_as_uint(b), false, false);
  return __uint_as_float(s[0]) + __uint_as_float(s[1]);
}
__device__ __forceinline__ float tr8(float a, float b, bool up) {
  const float keep = up ? b : a, send = up ? a : b;
  return keep + dpp<XOR8>(send);
}
__device__ __forceinline__ float max32(float x) {
  auto s = __builtin_amdgcn_permlane32_swap(__float_as_uint(x), __float_as_uint(x), false, false);
  return fmaxf(__uint_as_float(s[0]), __uint_as_float(s[1]));
}
__device__ __forceinline__ float max16(float x) {
  auto s = __builtin_amdgcn_permlane16_swap(__float_as_uint(x), __float_as_uint(x), false, false);
  return fmaxf(__uint_as_float(s[0]), __uint_as_float(s[1]));
}
__device__ __forceinline__ void reduce24(float (&v)[24], int lane, float* dst  ) {
#pragma unroll
  for (int k = 0; k < 12; ++k) v[k] = tr32(v[k], v[k + 12]);
#pragma unroll
  for (int k = 0; k < 6; ++k) v[k] = tr16(v[k], v[k + 6]);
  const bool up = lane & 8;
#pragma unroll
  for (int k = 0; k < 3; ++k) v[k] = tr8(v[k], v[k + 3], up);
  const int base = 3 * (lane >> 3), j = lane & 7;
#pragma unroll
  for (int k = 0; k < 3; ++k) { const int idx = base + k; dst[(idx >> 3) * 64 + 8 * j + (idx & 7)] = v[k]; }
}
#define PACE() do { asm volatile("s_waitcnt vmcnt(%0)" :: "n"(WIN - 1) : "memory"); __builtin_amdgcn_sched_barrier(0); } while (0)
#define PIN() __builtin_amdgcn_sched_barrier(0)

template <bool HAS_KV>
__device__ __forceinline__ void stream_role(const u16* __restrict__ x, const u16* __restrict__ kc, const u16* __restrict__ vc,
    const u16* __restrict__ wq, const u16* __restrict__ wo, int h, int s, int wave, int lane,
    float (*red)[192], float (*red2)[80], const unsigned* qs, const float* os, float (*wml)[2], unsigned char* ws) {
  const int rp = lane >> 3, j = lane & 7;
  const int qrow = 80 * s + 8 * wave + rp;
  const unsigned xr = x[qrow];
  PIN();
  v4u WQ[3], KK[2][3], VV[2][3], WO[6];
#pragma unroll
  for (int i = 0; i < 3; ++i) WQ[i] = ldnt(wq + (size_t)qrow * (HQ * DH) + h * DH + 64 * i + 8 * j);
  const size_t kvbase = ((size_t)h * SK + (SK - W) + KPS * s + 16 * wave + rp) * DH + 8 * j;
  PIN();
  if constexpr (HAS_KV) {
#pragma unroll
    for (int i = 0; i < 3; ++i) { PACE(); VV[0][i] = ldnt(vc + kvbase + 64 * i); PIN(); }
  }
  {
    const float xv = __uint_as_float(xr << 16);
    float a[24];
#pragma unroll
    for (int i = 0; i < 3; ++i)
#pragma unroll
      for (int e = 0; e < 4; ++e) { a[8 * i + 2 * e] = xv * bflo(WQ[i][e]); a[8 * i + 2 * e + 1] = xv * bfhi(WQ[i][e]); }
    reduce24(a, lane, red[wave]);
  }
  __syncthreads();
  const int r4 = lane >> 4, cc = lane & 15;
  if constexpr (HAS_KV) {
#pragma unroll
    for (int i = 0; i < 3; ++i) { PACE(); VV[1][i] = ldnt(vc + kvbase + (size_t)8 * DH + 64 * i); PIN(); }
#pragma unroll
    for (int i = 0; i < 3; ++i) { PACE(); KK[0][i] = ldnt(kc + kvbase + 64 * i); PIN(); }
#pragma unroll
    for (int i = 0; i < 3; ++i) { PACE(); KK[1][i] = ldnt(kc + kvbase + (size_t)8 * DH + 64 * i); PIN(); }
    const u16* wb = wo + (size_t)(h * DH + 24 * wave + r4) * D + 80 * s + 8 * (cc < 10 ? cc : 9);
#pragma unroll
    for (int i = 0; i < 6; ++i) { PACE(); WO[i] = ldnt(wb + (size_t)(4 * i) * D); PIN(); }
  }
  if (wave == 0) ST(12);
  __syncthreads();
  if constexpr (HAS_KV) {
    v4u qp[3];
#pragma unroll
    for (int i = 0; i < 3; ++i) qp[i] = *(const v4u*)&qs[32 * i + 4 * j];
    float sv[2];
#pragma unroll
    for (int g = 0; g < 2; ++g) {
      float d = 0.f;
#pragma unroll
      for (int i = 0; i < 3; ++i)
#pragma unroll
        for (int e = 0; e < 4; ++e) d = dot2(KK[g][i][e], qp[i][e], d);
      d += dpp<XOR1>(d); d += dpp<XOR2>(d); d += dpp<XOR7>(d);
      sv[g] = rbf(rbf(d) * 0.07216878364870322f);
    }
    float m = fmaxf(sv[0], sv[1]);
    m = fmaxf(m, dpp<XOR8>(m)); m = max16(m); m = max32(m);
    const float p0 = __expf(sv[0] - m), p1 = __expf(sv[1] - m);
    float l = p0 + p1;
    l += dpp<XOR8>(l); l = tr16(l, l); l = tr32(l, l);
    float a[24];
#pragma unroll
    for (int i = 0; i < 3; ++i)
#pragma unroll
      for (int e = 0; e < 4; ++e) {
        a[8 * i + 2 * e] = p0 * bflo(VV[0][i][e]) + p1 * bflo(VV[1][i][e]);
        a[8 * i + 2 * e + 1] = p0 * bfhi(VV[0][i][e]) + p1 * bfhi(VV[1][i][e]);
      }
    reduce24(a, lane, red[wave]);
    if (lane == 0) { wml[wave][0] = m; wml[wave][1] = l; }
  }
  __syncthreads();
  __syncthreads();
  if constexpr (HAS_KV) {
    float a[8];
#pragma unroll
    for (int e = 0; e < 8; ++e) a[e] = 0.f;
#pragma unroll
    for (int i = 0; i < 6; ++i) {
      const float ov = os[24 * wave + 4 * i + r4];
#pragma unroll
      for (int e = 0; e < 4; ++e) { a[2 * e] += ov * bflo(WO[i][e]); a[2 * e + 1] += ov * bfhi(WO[i][e]); }
    }
#pragma unroll
    for (int k = 0; k < 4; ++k) a[k] = tr32(a[k], a[k + 4]);
#pragma unroll
    for (int k = 0; k < 2; ++k) a[k] = tr16(a[k], a[k + 2]);
    if (cc < 10) *(float2*)&red2[wave][8 * cc + 2 * (r4 & 1) + 4 * (r4 >> 1)] = make_float2(a[0], a[1]);
  }
}

__device__ __forceinline__ void gather8(__amdgpu_buffer_rsrc_t rs, unsigned base, int lane, int last, unsigned A, unsigned B,
                                        v4u (&g0)[8], v4u (&g1)[8]) {
  const unsigned o1 = 1024 + (lane < 32 ? lane : last) * 16;
  for (unsigned spin = 0; spin < SPIN_CAP; ++spin) {
    asm volatile("" ::: "memory");
    bool ok = true;
#pragma unroll
    for (int p = 0; p < 8; ++p) {
      g0[p] = __builtin_amdgcn_raw_buffer_load_b128(rs, base + p * 2048 + lane * 16, 0, 16);
      g1[p] = __builtin_amdgcn_raw_buffer_load_b128(rs, base + p * 2048 + o1, 0, 16);
    }
#pragma unroll
    for (int p = 0; p < 8; ++p) ok = ok & okgran(g0[p], A, B) & okgran(g1[p], A, B);
    if (__all(ok)) break;
    __builtin_amdgcn_s_sleep(1);
  }
}

__global__ void __launch_bounds__(NT, 1)
fused_decode(const u16* __restrict__ x, const u16* __restrict__ kc, const u16* __restrict__ vc,
             const u16* __restrict__ wq, const u16* __restrict__ wo, u16* __restrict__ out, unsigned char* ws) {
  __shared__ __attribute__((aligned(16))) float red[NWS][192];
  __shared__ __attribute__((aligned(16))) float red2[8][80];
  __shared__ __attribute__((aligned(16))) unsigned qs[96];
  __shared__ __attribute__((aligned(16))) float os[192];
  __shared__ __attribute__((aligned(16))) float wml[8][2];

  const int b = blockIdx.x;
  const int h = (b & 7) * 4 + ((b >> 3) & 3), s = b >> 5;
  const int tid = threadIdx.x, lane = tid & 63;
  const int wave = __builtin_amdgcn_readfirstlane(tid >> 6);

  if (wave >= 1 && wave <= 8) {
    stream_role<true>(x, kc, vc, wq, wo, h, s, wave - 1, lane, red, red2, qs, os, wml, ws);
    __syncthreads();
  } else if (wave > 8) {
    stream_role<false>(x, kc, vc, wq, wo, h, s, wave - 1, lane, red, red2, qs, os, wml, ws);
    __syncthreads();
  } else {
    const __amdgpu_buffer_rsrc_t rs = __builtin_amdgcn_make_buffer_rsrc(ws, 0, WS_END, 0x00020000);
    ST(0);
    const unsigned long long E = __hip_atomic_load((unsigned long long*)(ws + WS_E), __ATOMIC_RELAXED, __HIP_MEMORY_SCOPE_AGENT);
    const unsigned long long T = E + 1ull;
    const unsigned A = (unsigned)T, B = (unsigned)(T >> 32) ^ ((unsigned)T * 0x9E3779B9u);
    ST(1);
    __syncthreads();
    ST(2);
    {
      float s0 = 0.f, s1 = 0.f, t0 = 0.f, t1 = 0.f;
#pragma unroll
      for (int w = 0; w < NWS; ++w) {
        const float2 u = *(const float2*)&red[w][2 * lane];
        s0 += u.x; s1 += u.y;
        const float2 v = *(const float2*)&red[w][128 + 2 * (lane & 31)];
        t0 += v.x; t1 += v.y;
      }
      const unsigned off = WS_QX + (unsigned)(h * 8 + s) * 2048u;
      __builtin_amdgcn_raw_buffer_store_b128(mkgran(s0, s1, A, B), rs, off + lane * 16, 0, 16);
      if (lane < 32) __builtin_amdgcn_raw_buffer_store_b128(mkgran(t0, t1, A, B), rs, off + 1024 + lane * 16, 0, 16);
    }
    ST(3);
    {
      v4u g0[8], g1[8];
      gather8(rs, WS_QX + (unsigned)h * 16384u, lane, 0, A, B, g0, g1);
      float s0 = 0.f, s1 = 0.f, t0 = 0.f, t1 = 0.f;
#pragma unroll
      for (int p = 0; p < 8; ++p) {
        s0 += __uint_as_float(g0[p][0]); s1 += __uint_as_float(g0[p][1]);
        t0 += __uint_as_float(g1[p][0]); t1 += __uint_as_float(g1[p][1]);
      }
      qs[lane] = (__float_as_uint(rbf(s0)) >> 16) | __float_as_uint(rbf(s1));
      if (lane < 32) qs[64 + lane] = (__float_as_uint(rbf(t0)) >> 16) | __float_as_uint(rbf(t1));
    }
    ST(4);
    __syncthreads();
    __syncthreads();
    ST(5);
    {
      float M = wml[0][0];
#pragma unroll
      for (int w = 1; w < 8; ++w) M = fmaxf(M, wml[w][0]);
      float s0 = 0.f, s1 = 0.f, t0 = 0.f, t1 = 0.f, L = 0.f;
#pragma unroll
      for (int w = 0; w < 8; ++w) {
        const float c = __expf(wml[w][0] - M);
        L += c * wml[w][1];
        const float2 u = *(const float2*)&red[w][2 * lane];
        s0 += c * u.x; s1 += c * u.y;
        const float2 v = *(const float2*)&red[w][128 + 2 * (lane & 31)];
        t0 += c * v.x; t1 += c * v.y;
      }
      if (lane >= 32) { t0 = M; t1 = L; }
      const unsigned off = WS_OX + (unsigned)(h * 8 + s) * 2048u;
      __builtin_amdgcn_raw_buffer_store_b128(mkgran(s0, s1, A, B), rs, off + lane * 16, 0, 16);
      if (lane <= 32) __builtin_amdgcn_raw_buffer_store_b128(mkgran(t0, t1, A, B), rs, off + 1024 + lane * 16, 0, 16);
    }
    ST(6);
    {
      v4u g0[8], g1[8];
      gather8(rs, WS_OX + (unsigned)h * 16384u, lane, 32, A, B, g0, g1);
      float mp[8], M = -INFINITY;
#pragma unroll
      for (int p = 0; p < 8; ++p) { mp[p] = __uint_as_float(__builtin_amdgcn_readlane(g1[p][0], 32)); M = fmaxf(M, mp[p]); }
      float s0 = 0.f, s1 = 0.f, t0 = 0.f, t1 = 0.f, L = 0.f;
#pragma unroll
      for (int p = 0; p < 8; ++p) {
        const float c = __expf(mp[p] - M);
        L += c * __uint_as_float(__builtin_amdgcn_readlane(g1[p][1], 32));
        s0 += c * __uint_as_float(g0[p][0]); s1 += c * __uint_as_float(g0[p][1]);
        t0 += c * __uint_as_float(g1[p][0]); t1 += c * __uint_as_float(g1[p][1]);
      }
      const float inv = 1.0f / L;
      *(float2*)&os[2 * lane] = make_float2(rbf(s0 * inv), rbf(s1 * inv));
      if (lane < 32) *(float2*)&os[128 + 2 * lane] = make_float2(rbf(t0 * inv), rbf(t1 * inv));
    }
    ST(7);
    __syncthreads();
    __syncthreads();
    ST(8);
    if (lane < 40) {
      float o0 = 0.f, o1 = 0.f;
#pragma unroll
      for (int w = 0; w < 8; ++w) { const float2 u = *(const float2*)&red2[w][2 * lane]; o0 += u.x; o1 += u.y; }
      __builtin_amdgcn_raw_buffer_store_b128(mkgran(o0, o1, A, B), rs,
          WS_PX + (unsigned)(s * 5 + (lane >> 3)) * 4096u + (unsigned)h * 128u + (lane & 7) * 16, 0, 16);
    }
    if (b < NRED) {
      v4u g[4];
      const unsigned off = WS_PX + (unsigned)b * 4096u + (unsigned)lane * 16u;
      for (unsigned spin = 0; spin < SPIN_CAP; ++spin) {
        asm volatile("" ::: "memory");
        bool ok = true;
#pragma unroll
        for (int k = 0; k < 4; ++k) g[k] = __builtin_amdgcn_raw_buffer_load_b128(rs, off + k * 1024, 0, 16);
#pragma unroll
        for (int k = 0; k < 4; ++k) ok = ok & okgran(g[k], A, B);
        if (__all(ok)) break;
        __builtin_amdgcn_s_sleep(1);
      }
      ST(9);
      float v0 = (__uint_as_float(g[0][0]) + __uint_as_float(g[1][0])) + (__uint_as_float(g[2][0]) + __uint_as_float(g[3][0]));
      float v1 = (__uint_as_float(g[0][1]) + __uint_as_float(g[1][1])) + (__uint_as_float(g[2][1]) + __uint_as_float(g[3][1]));
      v0 = tr32(v0, v1);
      v0 = tr16(v0, v0);
      v0 += dpp<XOR8>(v0);
      if ((lane & 24) == 0) out[b * 16 + 2 * (lane & 7) + (lane >> 5)] = (u16)(__float_as_uint(rbf(v0)) >> 16);
      if (b == 0 && lane == 8) {
        __hip_atomic_store((unsigned long long*)(ws + WS_E), T, __ATOMIC_RELAXED, __HIP_MEMORY_SCOPE_AGENT);
      }
      ST(10);
    }
  }
}

extern "C" void kernel_launch(void* const* d_in, const int* in_sizes, int n_in,
                              void* d_out, int out_size, void* d_ws, size_t ws_size,
                              hipStream_t stream) {
  fused_decode<<<256, NT, 0, stream>>>((const u16*)d_in[0], (const u16*)d_in[1], (const u16*)d_in[2],
                                       (const u16*)d_in[3], (const u16*)d_in[4], (u16*)d_out, (unsigned char*)d_ws);
}
```

```cpp
#include <hip/hip_runtime.h>
#include <hip/hip_bf16.h>
#include <stdint.h>

constexpr int D = 640, HQ = 32, DH = 192, SK = 4096, W = 1024, NS = 8, KPS = W / NS;
constexpr int NWS = 10;
constexpr int NT = 64 * (NWS + 1);
constexpr int NRED = 40;

typedef unsigned v4u __attribute__((ext_vector_type(4)));
typedef __attribute__((ext_vector_type(2))) __bf16 bf16x2;
typedef unsigned short u16;

constexpr unsigned WS_E = 0;
constexpr unsigned WS_QX = 4096;
constexpr unsigned WS_OX = WS_QX + 256 * 2048;
constexpr unsigned WS_PX = WS_OX + 256 * 2048;
constexpr unsigned WS_END = WS_PX + NRED * 4096;
#define WOPACE 0
#define REFINED 1
#define QDELAY 40
#define WIN 3
constexpr unsigned SPIN_CAP = 1u << 16;
#define ST(k)

__device__ __forceinline__ float bflo(unsigned u) { return __uint_as_float(u << 16); }
__device__ __forceinline__ float bfhi(unsigned u) { return __uint_as_float(u & 0xffff0000u); }
__device__ __forceinline__ float rbf(float f) {
  unsigned u = __float_as_uint(f);
  u = (u + 0x7fffu + ((u >> 16) & 1u)) & 0xffff0000u;
  return __uint_as_float(u);
}
__device__ __forceinline__ float dot2(unsigned k, unsigned q, float acc) {
  return __builtin_amdgcn_fdot2_f32_bf16(__builtin_bit_cast(bf16x2, k), __builtin_bit_cast(bf16x2, q), acc, false);
}
__device__ __forceinline__ v4u ldnt(const u16* p) { return __builtin_nontemporal_load((const v4u*)p); }
__device__ __forceinline__ v4u mkgran(float a, float b, unsigned A, unsigned B) {
  const unsigned v0 = __float_as_uint(a), v1 = __float_as_uint(b);
  v4u g; g[0] = v0; g[1] = v1; g[2] = v0 ^ A; g[3] = v1 ^ B;
  return g;
}
__device__ __forceinline__ unsigned xor3(unsigned a, unsigned b, unsigned c) {
  unsigned r; asm("v_bitop3_b32 %0, %1, %2, %3 bitop3:0x96" : "=v"(r) : "v"(a), "v"(b), "v"(c)); return r;
}
__device__ __forceinline__ unsigned or3(unsigned a, unsigned b, unsigned c) {
  unsigned r; asm("v_or3_b32 %0, %1, %2, %3" : "=v"(r) : "v"(a), "v"(b), "v"(c)); return r;
}
__device__ __forceinline__ unsigned badgran(unsigned bad, v4u g, unsigned A, unsigned B) {
  return or3(bad, xor3(g[0], g[2], A), xor3(g[1], g[3], B));
}
__device__ __forceinline__ float dot2z(unsigned k, unsigned q) {
  float r; asm("v_dot2_f32_bf16 %0, %1, %2, 0" : "=v"(r) : "v"(k), "v"(q)); return r;
}
__device__ __forceinline__ unsigned pkbf(float lo, float hi) {
  unsigned r; asm("v_cvt_pk_bf16_f32 %0, %1, %2" : "=v"(r) : "v"(lo), "v"(hi)); return r;
}
__device__ __forceinline__ unsigned plo(unsigned a, unsigned b) { return __builtin_amdgcn_perm(b, a, 0x05040100u); }
__device__ __forceinline__ unsigned phi(unsigned a, unsigned b) { return __builtin_amdgcn_perm(b, a, 0x07060302u); }
template <int CTRL> __device__ __forceinline__ float dpp(float x) {
  return __builtin_bit_cast(float, __builtin_amdgcn_mov_dpp(__builtin_bit_cast(int, x), CTRL, 0xf, 0xf, true));
}
constexpr int XOR1 = 0xB1, XOR2 = 0x4E, XOR7 = 0x141, XOR8 = 0x128;
__device__ __forceinline__ float tr32(float a, float b) {
  auto s = __builtin_amdgcn_permlane32_swap(__float_as_uint(a), __float_as_uint(b), false, false);
  return __uint_as_float(s[0]) + __uint_as_float(s[1]);
}
__device__ __forceinline__ float tr16(float a, float b) {
  auto s = __builtin_amdgcn_permlane16_swap(__float_as_uint(a), __float_as_uint(b), false, false);
  return __uint_as_float(s[0]) + __uint_as_float(s[1]);
}
__device__ __forceinline__ float tr8(float a, float b, bool up) {
  const float keep = up ? b : a, send = up ? a : b;
  return keep + dpp<XOR8>(send);
}
__device__ __forceinline__ float max32(float x) {
  auto s = __builtin_amdgcn_permlane32_swap(__float_as_uint(x), __float_as_uint(x), false, false);
  return fmaxf(__uint_as_float(s[0]), __uint_as_float(s[1]));
}
__device__ __forceinline__ float max16(float x) {
  auto s = __builtin_amdgcn_permlane16_swap(__float_as_uint(x), __float_as_uint(x), false, false);
  return fmaxf(__uint_as_float(s[0]), __uint_as_float(s[1]));
}
__device__ __forceinline__ void reduce24(float (&v)[24], int lane, float* dst  ) {
#pragma unroll
  for (int k = 0; k < 12; ++k) v[k] = tr32(v[k], v[k + 12]);
#pragma unroll
  for (int k = 0; k < 6; ++k) v[k] = tr16(v[k], v[k + 6]);
  const bool up = lane & 8;
#pragma unroll
  for (int k = 0; k < 3; ++k) v[k] = tr8(v[k], v[k + 3], up);
  const int base = 3 * (lane >> 3), j = lane & 7;
#pragma unroll
  for (int k = 0; k < 3; ++k) { const int idx = base + k; dst[(idx >> 3) * 64 + 8 * j + (idx & 7)] = v[k]; }
}
#define PACE() do { asm volatile("s_waitcnt vmcnt(%0)" :: "n"(WIN - 1) : "memory"); __builtin_amdgcn_sched_barrier(0); } while (0)
#define PIN() __builtin_amdgcn_sched_barrier(0)

template <bool HAS_KV>
__device__ __forceinline__ void stream_role(const u16* __restrict__ x, const u16* __restrict__ kc, const u16* __restrict__ vc,
    const u16* __restrict__ wq, const u16* __restrict__ wo, int h, int s, int wave, int lane,
    float (*red)[192], float (*red2)[80], const unsigned* qs, const float* os, float (*wml)[2], unsigned char* ws) {
  const int rp = lane >> 3, j = lane & 7;
  const int qrow = 80 * s + 8 * wave + rp;
  const unsigned xr = x[qrow];
  PIN();
  v4u WQ[3], KK[2][3], VV[2][3], WO[6];
#pragma unroll
  for (int i = 0; i < 3; ++i) WQ[i] = ldnt(wq + (size_t)qrow * (HQ * DH) + h * DH + 64 * i + 8 * j);
  const size_t kvbase = ((size_t)h * SK + (SK - W) + KPS * s + 16 * wave + rp) * DH + 8 * j;
  PIN();
  if constexpr (HAS_KV) {
#pragma unroll
    for (int i = 0; i < 3; ++i) { PACE(); VV[0][i] = ldnt(vc + kvbase + 64 * i); PIN(); }
  }
  {
    const float xv = __uint_as_float(xr << 16);
    float a[24];
#pragma unroll
    for (int i = 0; i < 3; ++i)
#pragma unroll
      for (int e = 0; e < 4; ++e) { a[8 * i + 2 * e] = xv * bflo(WQ[i][e]); a[8 * i + 2 * e + 1] = xv * bfhi(WQ[i][e]); }
    reduce24(a, lane, red[wave]);
  }
  __syncthreads();
  const int r4 = lane >> 4, cc = lane & 15;
  __syncthreads();
  PIN();
  if constexpr (HAS_KV) {
    const u16* wb = wo + (size_t)(h * DH + 24 * wave + r4) * D + 80 * s + 8 * (cc < 10 ? cc : 9);
#pragma unroll
    for (int i = 0; i < 3; ++i) VV[1][i] = ldnt(vc + kvbase + (size_t)8 * DH + 64 * i);
    PIN();
#pragma unroll
    for (int g = 0; g < 2; ++g)
#pragma unroll
      for (int i = 0; i < 3; ++i) KK[g][i] = ldnt(kc + kvbase + (size_t)g * 8 * DH + 64 * i);
    PIN();
    asm volatile("s_waitcnt vmcnt(6)" ::: "memory"); PIN();
    __syncthreads();
    __syncthreads();
    PIN();
#pragma unroll
    for (int i = 0; i < 6; ++i) WO[i] = ldnt(wb + (size_t)(4 * i) * D);
    PIN();
  } else {
    __syncthreads();
    __syncthreads();
  }
  if (wave == 0) ST(12);
  __syncthreads();
  if constexpr (HAS_KV) {
    v4u qp[3];
#pragma unroll
    for (int i = 0; i < 3; ++i) qp[i] = *(const v4u*)&qs[32 * i + 4 * j];
    float sv[2];
#pragma unroll
    for (int g = 0; g < 2; ++g) {
      float d = 0.f;
#pragma unroll
      for (int i = 0; i < 3; ++i)
#pragma unroll
        for (int e = 0; e < 4; ++e) d = dot2(KK[g][i][e], qp[i][e], d);
      d += dpp<XOR1>(d); d += dpp<XOR2>(d); d += dpp<XOR7>(d);
      sv[g] = rbf(rbf(d) * 0.07216878364870322f);
    }
    float m = fmaxf(sv[0], sv[1]);
    m = fmaxf(m, dpp<XOR8>(m)); m = max16(m); m = max32(m);
    const float p0 = __expf(sv[0] - m), p1 = __expf(sv[1] - m);
    float l = p0 + p1;
    l += dpp<XOR8>(l); l = tr16(l, l); l = tr32(l, l);
    float a[24];
    const unsigned pp = pkbf(p0, p1);
#pragma unroll
    for (int i = 0; i < 3; ++i)
#pragma unroll
      for (int e = 0; e < 4; ++e) {
        a[8 * i + 2 * e] = dot2z(plo(VV[0][i][e], VV[1][i][e]), pp);
        a[8 * i + 2 * e + 1] = dot2z(phi(VV[0][i][e], VV[1][i][e]), pp);
      }
    reduce24(a, lane, red[wave]);
    if (lane == 0) { wml[wave][0] = m; wml[wave][1] = l; }
  }
  __syncthreads();
  __syncthreads();
  if constexpr (HAS_KV) {
    float a[8];
#pragma unroll
    for (int e = 0; e < 8; ++e) a[e] = 0.f;
#pragma unroll
    for (int i = 0; i < 6; i += 2) {
      const unsigned op = __float_as_uint(os[24 * wave + 4 * i + r4]) >> 16 | __float_as_uint(os[24 * wave + 4 * i + 4 + r4]);
#pragma unroll
      for (int e = 0; e < 4; ++e) {
        a[2 * e] = dot2(plo(WO[i][e], WO[i + 1][e]), op, a[2 * e]);
        a[2 * e + 1] = dot2(phi(WO[i][e], WO[i + 1][e]), op, a[2 * e + 1]);
      }
    }
#pragma unroll
    for (int k = 0; k < 4; ++k) a[k] = tr32(a[k], a[k + 4]);
#pragma unroll
    for (int k = 0; k < 2; ++k) a[k] = tr16(a[k], a[k + 2]);
    if (cc < 10) *(float2*)&red2[wave][8 * cc + 2 * (r4 & 1) + 4 * (r4 >> 1)] = make_float2(a[0], a[1]);
  }
}

template <bool BAR>
__device__ __forceinline__ void gather8(__amdgpu_buffer_rsrc_t rs, unsigned base, int lane, int last, unsigned A, unsigned B,
                                        v4u (&g0)[8], v4u (&g1)[8]) {
  const unsigned o1 = 1024 + (lane < 32 ? lane : last) * 16;
  for (unsigned spin = 0; spin < SPIN_CAP; ++spin) {
    asm volatile("" ::: "memory");
    unsigned bad = 0;
#pragma unroll
    for (int p = 0; p < 8; ++p) {
      g0[p] = __builtin_amdgcn_raw_buffer_load_b128(rs, base + p * 2048 + lane * 16, 0, 16);
      g1[p] = __builtin_amdgcn_raw_buffer_load_b128(rs, base + p * 2048 + o1, 0, 16);
    }
    if (BAR && spin == 0) { PIN(); __builtin_amdgcn_s_barrier(); PIN(); }
#pragma unroll
    for (int p = 0; p < 8; ++p) bad = badgran(badgran(bad, g0[p], A, B), g1[p], A, B);
    if (__all(bad == 0)) break;
    __builtin_amdgcn_s_sleep(1);
  }
}

__global__ void __launch_bounds__(NT, 1)
fused_decode(const u16* __restrict__ x, const u16* __restrict__ kc, const u16* __restrict__ vc,
             const u16* __restrict__ wq, const u16* __restrict__ wo, u16* __restrict__ out, unsigned char* ws) {
  __shared__ __attribute__((aligned(16))) float red[NWS][192];
  __shared__ __attribute__((aligned(16))) float red2[8][80];
  __shared__ __attribute__((aligned(16))) unsigned qs[96];
  __shared__ __attribute__((aligned(16))) float os[192];
  __shared__ __attribute__((aligned(16))) float wml[8][2];

  const int b = blockIdx.x;
  const int h = (b & 7) * 4 + ((b >> 3) & 3), s = b >> 5;
  const int tid = threadIdx.x, lane = tid & 63;
  const int wave = __builtin_amdgcn_readfirstlane(tid >> 6);

  if (wave >= 1 && wave <= 8) {
    stream_role<true>(x, kc, vc, wq, wo, h, s, wave - 1, lane, red, red2, qs, os, wml, ws);
    __syncthreads();
  } else if (wave > 8) {
    stream_role<false>(x, kc, vc, wq, wo, h, s, wave - 1, lane, red, red2, qs, os, wml, ws);
    __syncthreads();
  } else {
    const __amdgpu_buffer_rsrc_t rs = __builtin_amdgcn_make_buffer_rsrc(ws, 0, WS_END, 0x00020000);
    ST(0);
    const unsigned long long E = __hip_atomic_load((unsigned long long*)(ws + WS_E), __ATOMIC_RELAXED, __HIP_MEMORY_SCOPE_AGENT);
    const unsigned long long T = E + 1ull;
    const unsigned A = 2u * (unsigned)T + 1u, B = (unsigned)(T >> 31) ^ ((unsigned)T * 0x9E3779B9u);
    ST(1);
    __syncthreads();
    ST(2);
    {
      float s0 = 0.f, s1 = 0.f, t0 = 0.f, t1 = 0.f;
#pragma unroll
      for (int w = 0; w < NWS; ++w) {
        const float2 u = *(const float2*)&red[w][2 * lane];
        s0 += u.x; s1 += u.y;
        const float2 v = *(const float2*)&red[w][128 + 2 * (lane & 31)];
        t0 += v.x; t1 += v.y;
      }
      const unsigned off = WS_QX + (unsigned)(h * 8 + s) * 2048u;
      __builtin_amdgcn_raw_buffer_store_b128(mkgran(s0, s1, A, B), rs, off + lane * 16, 0, 16);
      if (lane < 32) __builtin_amdgcn_raw_buffer_store_b128(mkgran(t0, t1, A, B), rs, off + 1024 + lane * 16, 0, 16);
    }
    PIN();
    __syncthreads();
    ST(3);
    {
      v4u g0[8], g1[8];
      __syncthreads();
      ST(11);
      gather8<REFINED>(rs, WS_QX + (unsigned)h * 16384u, lane, 0, A, B, g0, g1);
      float s0 = 0.f, s1 = 0.f, t0 = 0.f, t1 = 0.f;
#pragma unroll
      for (int p = 0; p < 8; ++p) {
        s0 += __uint_as_float(g0[p][0]); s1 += __uint_as_float(g0[p][1]);
        t0 += __uint_as_float(g1[p][0]); t1 += __uint_as_float(g1[p][1]);
      }
      qs[lane] = pkbf(s0, s1);
      if (lane < 32) qs[64 + lane] = pkbf(t0, t1);
    }
    ST(4);
    __syncthreads();
    __syncthreads();
    ST(5);
    {
      float M = wml[0][0];
#pragma unroll
      for (int w = 1; w < 8; ++w) M = fmaxf(M, wml[w][0]);
      float s0 = 0.f, s1 = 0.f, t0 = 0.f, t1 = 0.f, L = 0.f;
#pragma unroll
      for (int w = 0; w < 8; ++w) {
        const float c = __expf(wml[w][0] - M);
        L += c * wml[w][1];
        const float2 u = *(const float2*)&red[w][2 * lane];
        s0 += c * u.x; s1 += c * u.y;
        const float2 v = *(const float2*)&red[w][128 + 2 * (lane & 31)];
        t0 += c * v.x; t1 += c * v.y;
      }
      if (lane >= 32) { t0 = M; t1 = L; }
      const unsigned off = WS_OX + (unsigned)(h * 8 + s) * 2048u;
      __builtin_amdgcn_raw_buffer_store_b128(mkgran(s0, s1, A, B), rs, off + lane * 16, 0, 16);
      if (lane <= 32) __builtin_amdgcn_raw_buffer_store_b128(mkgran(t0, t1, A, B), rs, off + 1024 + lane * 16, 0, 16);
    }
    ST(6);
    {
      v4u g0[8], g1[8];
      gather8<false>(rs, WS_OX + (unsigned)h * 16384u, lane, 32, A, B, g0, g1);
      float mp[8], M = -INFINITY;
#pragma unroll
      for (int p = 0; p < 8; ++p) { mp[p] = __uint_as_float(__builtin_amdgcn_readlane(g1[p][0], 32)); M = fmaxf(M, mp[p]); }
      float s0 = 0.f, s1 = 0.f, t0 = 0.f, t1 = 0.f, L = 0.f;
#pragma unroll
      for (int p = 0; p < 8; ++p) {
        const float c = __expf(mp[p] - M);
        L += c * __uint_as_float(__builtin_amdgcn_readlane(g1[p][1], 32));
        s0 += c * __uint_as_float(g0[p][0]); s1 += c * __uint_as_float(g0[p][1]);
        t0 += c * __uint_as_float(g1[p][0]); t1 += c * __uint_as_float(g1[p][1]);
      }
      const float inv = 1.0f / L;
      *(float2*)&os[2 * lane] = make_float2(rbf(s0 * inv), rbf(s1 * inv));
      if (lane < 32) *(float2*)&os[128 + 2 * lane] = make_float2(rbf(t0 * inv), rbf(t1 * inv));
    }
    ST(7);
    __syncthreads();
    __syncthreads();
    ST(8);
    if (lane < 40) {
      float o0 = 0.f, o1 = 0.f;
#pragma unroll
      for (int w = 0; w < 8; ++w) { const float2 u = *(const float2*)&red2[w][2 * lane]; o0 += u.x; o1 += u.y; }
      __builtin_amdgcn_raw_buffer_store_b128(mkgran(o0, o1, A, B), rs,
          WS_PX + (unsigned)(s * 5 + (lane >> 3)) * 4096u + (unsigned)h * 128u + (lane & 7) * 16, 0, 16);
    }
    if (b < NRED) {
      v4u g[4];
      const unsigned off = WS_PX + (unsigned)b * 4096u + (unsigned)lane * 16u;
      for (unsigned spin = 0; spin < SPIN_CAP; ++spin) {
        asm volatile("" ::: "memory");
        unsigned bad = 0;
#pragma unroll
        for (int k = 0; k < 4; ++k) g[k] = __builtin_amdgcn_raw_buffer_load_b128(rs, off + k * 1024, 0, 16);
#pragma unroll
        for (int k = 0; k < 4; ++k) bad = badgran(bad, g[k], A, B);
        if (__all(bad == 0)) break;
        __builtin_amdgcn_s_sleep(1);
      }
      ST(9);
      float v0 = (__uint_as_float(g[0][0]) + __uint_as_float(g[1][0])) + (__uint_as_float(g[2][0]) + __uint_as_float(g[3][0]));
      float v1 = (__uint_as_float(g[0][1]) + __uint_as_float(g[1][1])) + (__uint_as_float(g[2][1]) + __uint_as_float(g[3][1]));
      v0 = tr32(v0, v1);
      v0 = tr16(v0, v0);
      v0 += dpp<XOR8>(v0);
      if ((lane & 24) == 0) out[b * 16 + 2 * (lane & 7) + (lane >> 5)] = (u16)(__float_as_uint(rbf(v0)) >> 16);
      if (b == 0 && lane == 8) {
        __hip_atomic_store((unsigned long long*)(ws + WS_E), T, __ATOMIC_RELAXED, __HIP_MEMORY_SCOPE_AGENT);
      }
      ST(10);
    }
  }
}

extern "C" void kernel_launch(void* const* d_in, const int* in_sizes, int n_in,
                              void* d_out, int out_size, void* d_ws, size_t ws_size,
                              hipStream_t stream) {
  fused_decode<<<256, NT, 0, stream>>>((const u16*)d_in[0], (const u16*)d_in[1], (const u16*)d_in[2],
                                       (const u16*)d_in[3], (const u16*)d_in[4], (u16*)d_out, (unsigned char*)d_ws);
}
```

```cpp
#include <hip/hip_runtime.h>
#include <hip/hip_bf16.h>
#include <stdint.h>

constexpr int D = 640, HQ = 32, DH = 192, SK = 4096, W = 1024, NS = 8, KPS = W / NS;
constexpr int NWS = 10;
constexpr int NT = 64 * (NWS + 1);
constexpr int NRED = 40;

typedef unsigned v4u __attribute__((ext_vector_type(4)));
typedef __attribute__((ext_vector_type(2))) __bf16 bf16x2;
typedef unsigned short u16;

constexpr unsigned WS_E = 0;
constexpr unsigned WS_QX = 4096;
constexpr unsigned WS_OX = WS_QX + 256 * 2048;
constexpr unsigned WS_PX = WS_OX + 256 * 2048;
constexpr unsigned WS_END = WS_PX + NRED * 4096;
#define WOPACE 0
#define REFINED 1
#define QDELAY 40
#define FIRSTW 1
#define BXW 6
#define WIN 3
constexpr unsigned SPIN_CAP = 1u << 16;
#define ST(k)

__device__ __forceinline__ float bflo(unsigned u) { return __uint_as_float(u << 16); }
__device__ __forceinline__ float bfhi(unsigned u) { return __uint_as_float(u & 0xffff0000u); }
__device__ __forceinline__ float rbf(float f) {
  unsigned u = __float_as_uint(f);
  u = (u + 0x7fffu + ((u >> 16) & 1u)) & 0xffff0000u;
  return __uint_as_float(u);
}
__device__ __forceinline__ float dot2(unsigned k, unsigned q, float acc) {
  return __builtin_amdgcn_fdot2_f32_bf16(__builtin_bit_cast(bf16x2, k), __builtin_bit_cast(bf16x2, q), acc, false);
}
__device__ __forceinline__ v4u ldnt(const u16* p) { return __builtin_nontemporal_load((const v4u*)p); }
__device__ __forceinline__ v4u mkgran(float a, float b, unsigned A, unsigned B) {
  const unsigned v0 = __float_as_uint(a), v1 = __float_as_uint(b);
  v4u g; g[0] = v0; g[1] = v1; g[2] = v0 ^ A; g[3] = v1 ^ B;
  return g;
}
__device__ __forceinline__ unsigned xor3(unsigned a, unsigned b, unsigned c) {
  unsigned r; asm("v_bitop3_b32 %0, %1, %2, %3 bitop3:0x96" : "=v"(r) : "v"(a), "v"(b), "v"(c)); return r;
}
__device__ __forceinline__ unsigned or3(unsigned a, unsigned b, unsigned c) {
  unsigned r; asm("v_or3_b32 %0, %1, %2, %3" : "=v"(r) : "v"(a), "v"(b), "v"(c)); return r;
}
__device__ __forceinline__ unsigned badgran(unsigned bad, v4u g, unsigned A, unsigned B) {
  return or3(bad, xor3(g[0], g[2], A), xor3(g[1], g[3], B));
}
__device__ __forceinline__ float dot2z(unsigned k, unsigned q) {
  float r; asm("v_dot2_f32_bf16 %0, %1, %2, 0" : "=v"(r) : "v"(k), "v"(q)); return r;
}
__device__ __forceinline__ unsigned pkbf(float lo, float hi) {
  unsigned r; asm("v_cvt_pk_bf16_f32 %0, %1, %2" : "=v"(r) : "v"(lo), "v"(hi)); return r;
}
__device__ __forceinline__ unsigned plo(unsigned a, unsigned b) { return __builtin_amdgcn_perm(b, a, 0x05040100u); }
__device__ __forceinline__ unsigned phi(unsigned a, unsigned b) { return __builtin_amdgcn_perm(b, a, 0x07060302u); }
template <int CTRL> __device__ __forceinline__ float dpp(float x) {
  return __builtin_bit_cast(float, __builtin_amdgcn_mov_dpp(__builtin_bit_cast(int, x), CTRL, 0xf, 0xf, true));
}
__device__ __forceinline__ float rowbc12(float x) { return dpp<0x15C>(x); }
constexpr int XOR1 = 0xB1, XOR2 = 0x4E, XOR7 = 0x141, XOR8 = 0x128;
__device__ __forceinline__ float tr32(float a, float b) {
  auto s = __builtin_amdgcn_permlane32_swap(__float_as_uint(a), __float_as_uint(b), false, false);
  return __uint_as_float(s[0]) + __uint_as_float(s[1]);
}
__device__ __forceinline__ float tr16(float a, float b) {
  auto s = __builtin_amdgcn_permlane16_swap(__float_as_uint(a), __float_as_uint(b), false, false);
  return __uint_as_float(s[0]) + __uint_as_float(s[1]);
}
__device__ __forceinline__ float tr8(float a, float b, bool up) {
  const float keep = up ? b : a, send = up ? a : b;
  return keep + dpp<XOR8>(send);
}
__device__ __forceinline__ float max32(float x) {
  auto s = __builtin_amdgcn_permlane32_swap(__float_as_uint(x), __float_as_uint(x), false, false);
  return fmaxf(__uint_as_float(s[0]), __uint_as_float(s[1]));
}
__device__ __forceinline__ float max16(float x) {
  auto s = __builtin_amdgcn_permlane16_swap(__float_as_uint(x), __float_as_uint(x), false, false);
  return fmaxf(__uint_as_float(s[0]), __uint_as_float(s[1]));
}
__device__ __forceinline__ void reduce24(float (&v)[24], int lane, float* dst  ) {
#pragma unroll
  for (int k = 0; k < 12; ++k) v[k] = tr32(v[k], v[k + 12]);
#pragma unroll
  for (int k = 0; k < 6; ++k) v[k] = tr16(v[k], v[k + 6]);
  const bool up = lane & 8;
#pragma unroll
  for (int k = 0; k < 3; ++k) v[k] = tr8(v[k], v[k + 3], up);
  const int base = 3 * (lane >> 3), j = lane & 7;
#pragma unroll
  for (int k = 0; k < 3; ++k) { const int idx = base + k; dst[(idx >> 3) * 64 + 8 * j + (idx & 7)] = v[k]; }
}
#define PACE() do { asm volatile("s_waitcnt vmcnt(%0)" :: "n"(WIN - 1) : "memory"); __builtin_amdgcn_sched_barrier(0); } while (0)
#define PIN() __builtin_amdgcn_sched_barrier(0)

template <bool HAS_KV>
__device__ __forceinline__ void stream_role(const u16* __restrict__ x, const u16* __restrict__ kc, const u16* __restrict__ vc,
    const u16* __restrict__ wq, const u16* __restrict__ wo, int h, int s, int wave, int lane,
    float (*red)[192], float (*red2)[80], const unsigned* qs, float* os, float (*wml)[2], const unsigned* tag, unsigned char* ws) {
  const int rp = lane >> 3, j = lane & 7;
  const int qrow = 80 * s + 8 * wave + rp;
  const unsigned xr = x[qrow];
  PIN();
  v4u WQ[3], KK[2][3], VV[2][3], WO[6];
#pragma unroll
  for (int i = 0; i < 3; ++i) WQ[i] = ldnt(wq + (size_t)qrow * (HQ * DH) + h * DH + 64 * i + 8 * j);
  const size_t kvbase = ((size_t)h * SK + (SK - W) + KPS * s + 16 * wave + rp) * DH + 8 * j;
  PIN();
  if constexpr (HAS_KV) {
    asm volatile("s_waitcnt vmcnt(%0)" :: "n"(FIRSTW) : "memory"); PIN();
#pragma unroll
    for (int g = 0; g < 2; ++g)
#pragma unroll
      for (int i = 0; i < 3; ++i) KK[g][i] = ldnt(kc + kvbase + (size_t)g * 8 * DH + 64 * i);
    PIN();
  }
  {
    const float xv = __uint_as_float(xr << 16);
    float a[24];
#pragma unroll
    for (int i = 0; i < 3; ++i)
#pragma unroll
      for (int e = 0; e < 4; ++e) { a[8 * i + 2 * e] = xv * bflo(WQ[i][e]); a[8 * i + 2 * e + 1] = xv * bfhi(WQ[i][e]); }
    reduce24(a, lane, red[wave]);
  }
  __syncthreads();
  const int r4 = lane >> 4, cc = lane & 15;
  __syncthreads();
  PIN();
  if constexpr (HAS_KV) {
    const u16* wb = wo + (size_t)(h * DH + 24 * wave + r4) * D + 80 * s + 8 * (cc < 10 ? cc : 9);
#pragma unroll
    for (int g = 0; g < 2; ++g)
#pragma unroll
      for (int i = 0; i < 3; ++i) VV[g][i] = ldnt(vc + kvbase + (size_t)g * 8 * DH + 64 * i);
    PIN();
    asm volatile("s_waitcnt vmcnt(%0)" :: "n"(BXW) : "memory"); PIN();
    __syncthreads();
    __syncthreads();
    PIN();
#pragma unroll
    for (int i = 0; i < 6; ++i) WO[i] = ldnt(wb + (size_t)(4 * i) * D);
    PIN();
#pragma unroll
    for (int i = 0; i < 3; ++i)
#pragma unroll
      for (int e = 0; e < 4; ++e) {
        const unsigned lo = plo(VV[0][i][e], VV[1][i][e]), hi = phi(VV[0][i][e], VV[1][i][e]);
        VV[0][i][e] = lo; VV[1][i][e] = hi;
      }
#pragma unroll
    for (int i = 0; i < 3; ++i) asm volatile("" : "+v"(VV[0][i]), "+v"(VV[1][i]));
    PIN();
  } else {
    __syncthreads();
    __syncthreads();
  }
  if (wave == 0) ST(12);
  __syncthreads();
  if (wave == 0) ST(13);
  if constexpr (HAS_KV) {
    v4u qp[3];
#pragma unroll
    for (int i = 0; i < 3; ++i) qp[i] = *(const v4u*)&qs[32 * i + 4 * j];
    float sv[2];
#pragma unroll
    for (int g = 0; g < 2; ++g) {
      float dd[3];
#pragma unroll
      for (int i = 0; i < 3; ++i) {
        dd[i] = dot2z(KK[g][i][0], qp[i][0]);
#pragma unroll
        for (int e = 1; e < 4; ++e) dd[i] = dot2(KK[g][i][e], qp[i][e], dd[i]);
      }
      float d = (dd[0] + dd[1]) + dd[2];
      d += dpp<XOR1>(d); d += dpp<XOR2>(d); d += dpp<XOR7>(d);
      sv[g] = rbf(rbf(d) * 0.07216878364870322f);
    }
    float m = fmaxf(sv[0], sv[1]);
    m = fmaxf(m, dpp<XOR8>(m)); m = max16(m); m = max32(m);
    const float p0 = __expf(sv[0] - m), p1 = __expf(sv[1] - m);
    float l = p0 + p1;
    l += dpp<XOR8>(l); l = tr16(l, l); l = tr32(l, l);
    if (wave == 0) { asm volatile("" :: "v"(l)); ST(14); }
    float a[24];
    const unsigned pp = pkbf(p0, p1);
#pragma unroll
    for (int i = 0; i < 3; ++i)
#pragma unroll
      for (int e = 0; e < 4; ++e) {
        a[8 * i + 2 * e] = dot2z(VV[0][i][e], pp);
        a[8 * i + 2 * e + 1] = dot2z(VV[1][i][e], pp);
      }
    reduce24(a, lane, red[wave]);
    if (lane == 0) { wml[wave][0] = m; wml[wave][1] = l; }
  }
  if (wave == 0) ST(15);
  __syncthreads();
  PIN();
  if constexpr (HAS_KV) {
    const __amdgpu_buffer_rsrc_t rs = __builtin_amdgcn_make_buffer_rsrc(ws, 0, WS_END, 0x00020000);
    const unsigned A = tag[0], B = tag[1];
    const int gi = lane & 15;
    {
      float M = wml[0][0];
#pragma unroll
      for (int w = 1; w < 8; ++w) M = fmaxf(M, wml[w][0]);
      float s0 = 0.f, s1 = 0.f, L = 0.f;
#pragma unroll
      for (int w = 0; w < 8; ++w) {
        const float c = __expf(wml[w][0] - M);
        L += c * wml[w][1];
        const float2 u = *(const float2*)&red[w][24 * wave + 2 * (gi < 12 ? gi : 0)];
        s0 += c * u.x; s1 += c * u.y;
      }
      if (gi == 12) { s0 = M; s1 = L; }
      if (lane < 13)
        __builtin_amdgcn_raw_buffer_store_b128(mkgran(s0, s1, A, B), rs, WS_OX + (unsigned)((h * 8 + wave) * 8 + s) * 256u + lane * 16, 0, 16);
    }
    if (wave == 0) ST(6);
#pragma unroll
    for (int i = 0; i < 6; i += 2)
#pragma unroll
      for (int e = 0; e < 4; ++e) {
        const unsigned lo = plo(WO[i][e], WO[i + 1][e]), hi = phi(WO[i][e], WO[i + 1][e]);
        WO[i][e] = lo; WO[i + 1][e] = hi;
      }
#pragma unroll
    for (int i = 0; i < 6; ++i) asm volatile("" : "+v"(WO[i]));
    PIN();
    v4u g[2];
    const unsigned off = WS_OX + (unsigned)((h * 8 + wave) * 8) * 256u + (gi < 13 ? lane : (lane & 48)) * 16;
    for (unsigned spin = 0; spin < SPIN_CAP; ++spin) {
      asm volatile("" ::: "memory");
      g[0] = __builtin_amdgcn_raw_buffer_load_b128(rs, off, 0, 16);
      g[1] = __builtin_amdgcn_raw_buffer_load_b128(rs, off + 1024, 0, 16);
      const unsigned bad = badgran(badgran(0u, g[0], A, B), g[1], A, B);
      if (__all(bad == 0)) break;
      __builtin_amdgcn_s_sleep(1);
    }
    const float m0 = rowbc12(__uint_as_float(g[0][0])), l0 = rowbc12(__uint_as_float(g[0][1]));
    const float m1 = rowbc12(__uint_as_float(g[1][0])), l1 = rowbc12(__uint_as_float(g[1][1]));
    float M = max32(max16(fmaxf(m0, m1)));
    const float c0 = __expf(m0 - M), c1 = __expf(m1 - M);
    float L = c0 * l0 + c1 * l1;
    float o0 = c0 * __uint_as_float(g[0][0]) + c1 * __uint_as_float(g[1][0]);
    float o1 = c0 * __uint_as_float(g[0][1]) + c1 * __uint_as_float(g[1][1]);
    L = tr16(L, L); L = tr32(L, L);
    o0 = tr16(o0, o0); o0 = tr32(o0, o0);
    o1 = tr16(o1, o1); o1 = tr32(o1, o1);
    const float inv = 1.0f / L;
    if (lane < 12) *(float2*)&os[24 * wave + 2 * lane] = make_float2(rbf(o0 * inv), rbf(o1 * inv));
    if (wave == 0) ST(7);
    __builtin_amdgcn_wave_barrier();
    asm volatile("s_waitcnt lgkmcnt(0)" ::: "memory");
    float a[8];
#pragma unroll
    for (int e = 0; e < 8; ++e) a[e] = 0.f;
#pragma unroll
    for (int i = 0; i < 6; i += 2) {
      const unsigned op = __float_as_uint(os[24 * wave + 4 * i + r4]) >> 16 | __float_as_uint(os[24 * wave + 4 * i + 4 + r4]);
#pragma unroll
      for (int e = 0; e < 4; ++e) {
        a[2 * e] = dot2(WO[i][e], op, a[2 * e]);
        a[2 * e + 1] = dot2(WO[i + 1][e], op, a[2 * e + 1]);
      }
    }
#pragma unroll
    for (int k = 0; k < 4; ++k) a[k] = tr32(a[k], a[k + 4]);
#pragma unroll
    for (int k = 0; k < 2; ++k) a[k] = tr16(a[k], a[k + 2]);
    if (cc < 10) *(float2*)&red2[wave][8 * cc + 2 * (r4 & 1) + 4 * (r4 >> 1)] = make_float2(a[0], a[1]);
  }
}

template <bool BAR>
__device__ __forceinline__ void gather8(__amdgpu_buffer_rsrc_t rs, unsigned base, int lane, int last, unsigned A, unsigned B,
                                        v4u (&g0)[8], v4u (&g1)[8]) {
  const unsigned o1 = 1024 + (lane < 32 ? lane : last) * 16;
  for (unsigned spin = 0; spin < SPIN_CAP; ++spin) {
    asm volatile("" ::: "memory");
    unsigned bad = 0;
#pragma unroll
    for (int p = 0; p < 8; ++p) {
      g0[p] = __builtin_amdgcn_raw_buffer_load_b128(rs, base + p * 2048 + lane * 16, 0, 16);
      g1[p] = __builtin_amdgcn_raw_buffer_load_b128(rs, base + p * 2048 + o1, 0, 16);
    }
    if (BAR && spin == 0) { PIN(); __builtin_amdgcn_s_barrier(); PIN(); }
#pragma unroll
    for (int p = 0; p < 8; ++p) bad = badgran(badgran(bad, g0[p], A, B), g1[p], A, B);
    if (__all(bad == 0)) break;
    __builtin_amdgcn_s_sleep(1);
  }
}

__global__ void __launch_bounds__(NT, 1)
fused_decode(const u16* __restrict__ x, const u16* __restrict__ kc, const u16* __restrict__ vc,
             const u16* __restrict__ wq, const u16* __restrict__ wo, u16* __restrict__ out, unsigned char* ws) {
  __shared__ __attribute__((aligned(16))) float red[NWS][192];
  __shared__ __attribute__((aligned(16))) float red2[8][80];
  __shared__ __attribute__((aligned(16))) unsigned qs[96];
  __shared__ __attribute__((aligned(16))) float os[192];
  __shared__ __attribute__((aligned(16))) float wml[8][2];
  __shared__ unsigned tag[2];

  const int b = blockIdx.x;
  const int h = (b & 7) * 4 + ((b >> 3) & 3), s = b >> 5;
  const int tid = threadIdx.x, lane = tid & 63;
  const int wave = __builtin_amdgcn_readfirstlane(tid >> 6);

  if (wave >= 1 && wave <= 8) {
    stream_role<true>(x, kc, vc, wq, wo, h, s, wave - 1, lane, red, red2, qs, os, wml, tag, ws);
    __syncthreads();
  } else if (wave > 8) {
    stream_role<false>(x, kc, vc, wq, wo, h, s, wave - 1, lane, red, red2, qs, os, wml, tag, ws);
    __syncthreads();
  } else {
    const __amdgpu_buffer_rsrc_t rs = __builtin_amdgcn_make_buffer_rsrc(ws, 0, WS_END, 0x00020000);
    ST(0);
    const unsigned long long E = __hip_atomic_load((unsigned long long*)(ws + WS_E), __ATOMIC_RELAXED, __HIP_MEMORY_SCOPE_AGENT);
    const unsigned long long T = E + 1ull;
    const unsigned A = 2u * (unsigned)T + 1u, B = (unsigned)(T >> 31) ^ ((unsigned)T * 0x9E3779B9u);
    if (lane == 0) { tag[0] = A; tag[1] = B; }
    ST(1);
    __syncthreads();
    ST(2);
    {
      float s0 = 0.f, s1 = 0.f, t0 = 0.f, t1 = 0.f;
#pragma unroll
      for (int w = 0; w < NWS; ++w) {
        const float2 u = *(const float2*)&red[w][2 * lane];
        s0 += u.x; s1 += u.y;
        const float2 v = *(const float2*)&red[w][128 + 2 * (lane & 31)];
        t0 += v.x; t1 += v.y;
      }
      const unsigned off = WS_QX + (unsigned)(h * 8 + s) * 2048u;
      __builtin_amdgcn_raw_buffer_store_b128(mkgran(s0, s1, A, B), rs, off + lane * 16, 0, 16);
      if (lane < 32) __builtin_amdgcn_raw_buffer_store_b128(mkgran(t0, t1, A, B), rs, off + 1024 + lane * 16, 0, 16);
    }
    PIN();
    __syncthreads();
    ST(3);
    {
      v4u g0[8], g1[8];
      __syncthreads();
      ST(11);
      gather8<REFINED>(rs, WS_QX + (unsigned)h * 16384u, lane, 0, A, B, g0, g1);
      float s0 = 0.f, s1 = 0.f, t0 = 0.f, t1 = 0.f;
#pragma unroll
      for (int p = 0; p < 8; ++p) {
        s0 += __uint_as_float(g0[p][0]); s1 += __uint_as_float(g0[p][1]);
        t0 += __uint_as_float(g1[p][0]); t1 += __uint_as_float(g1[p][1]);
      }
      qs[lane] = pkbf(s0, s1);
      if (lane < 32) qs[64 + lane] = pkbf(t0, t1);
    }
    ST(4);
    __syncthreads();
    __syncthreads();
    ST(5);
    __syncthreads();
    ST(8);
    if (lane < 40) {
      float o0 = 0.f, o1 = 0.f;
#pragma unroll
      for (int w = 0; w < 8; ++w) { const float2 u = *(const float2*)&red2[w][2 * lane]; o0 += u.x; o1 += u.y; }
      __builtin_amdgcn_raw_buffer_store_b128(mkgran(o0, o1, A, B), rs,
          WS_PX + (unsigned)(s * 5 + (lane >> 3)) * 4096u + (unsigned)h * 128u + (lane & 7) * 16, 0, 16);
    }
    if (b < NRED) {
      v4u g[4];
      const unsigned off = WS_PX + (unsigned)b * 4096u + (unsigned)lane * 16u;
      for (unsigned spin = 0; spin < SPIN_CAP; ++spin) {
        asm volatile("" ::: "memory");
        unsigned bad = 0;
#pragma unroll
        for (int k = 0; k < 4; ++k) g[k] = __builtin_amdgcn_raw_buffer_load_b128(rs, off + k * 1024, 0, 16);
#pragma unroll
        for (int k = 0; k < 4; ++k) bad = badgran(bad, g[k], A, B);
        if (__all(bad == 0)) break;
        __builtin_amdgcn_s_sleep(1);
      }
      ST(9);
      float v0 = (__uint_as_float(g[0][0]) + __uint_as_float(g[1][0])) + (__uint_as_float(g[2][0]) + __uint_as_float(g[3][0]));
      float v1 = (__uint_as_float(g[0][1]) + __uint_as_float(g[1][1])) + (__uint_as_float(g[2][1]) + __uint_as_float(g[3][1]));
      v0 = tr32(v0, v1);
      v0 = tr16(v0, v0);
      v0 += dpp<XOR8>(v0);
      if ((lane & 24) == 0) out[b * 16 + 2 * (lane & 7) + (lane >> 5)] = (u16)(__float_as_uint(rbf(v0)) >> 16);
      if (b == 0 && lane == 8) {
        __hip_atomic_store((unsigned long long*)(ws + WS_E), T, __ATOMIC_RELAXED, __HIP_MEMORY_SCOPE_AGENT);
      }
      ST(10);
    }
  }
}

extern "C" void kernel_launch(void* const* d_in, const int* in_sizes, int n_in,
                              void* d_out, int out_size, void* d_ws, size_t ws_size,
                              hipStream_t stream) {
  fused_decode<<<256, NT, 0, stream>>>((const u16*)d_in[0], (const u16*)d_in[1], (const u16*)d_in[2],
                                       (const u16*)d_in[3], (const u16*)d_in[4], (u16*)d_out, (unsigned char*)d_ws);
}
```

```cpp
#include <hip/hip_runtime.h>
#include <hip/hip_bf16.h>
#include <stdint.h>

constexpr int D = 640, HQ = 32, DH = 192, SK = 4096, W = 1024, NS = 8, KPS = W / NS;
constexpr int NWS = 10;
constexpr int NT = 64 * (NWS + 1);
constexpr int NRED = 40;

typedef unsigned v4u __attribute__((ext_vector_type(4)));
typedef __attribute__((ext_vector_type(2))) __bf16 bf16x2;
typedef unsigned short u16;

constexpr unsigned WS_E = 0;
constexpr unsigned WS_QX = 4096;
constexpr unsigned WS_OX = WS_QX + 256 * 2048;
constexpr unsigned WS_PX = WS_OX + 256 * 2048;
constexpr unsigned WS_END = WS_PX + NRED * 4096;
constexpr unsigned SPIN_CAP = 1u << 16;

__device__ __forceinline__ float bflo(unsigned u) { return __uint_as_float(u << 16); }
__device__ __forceinline__ float bfhi(unsigned u) { return __uint_as_float(u & 0xffff0000u); }
__device__ __forceinline__ float rbf(float f) {
  unsigned u = __float_as_uint(f);
  u = (u + 0x7fffu + ((u >> 16) & 1u)) & 0xffff0000u;
  return __uint_as_float(u);
}
__device__ __forceinline__ float dot2(unsigned k, unsigned q, float acc) {
  return __builtin_amdgcn_fdot2_f32_bf16(__builtin_bit_cast(bf16x2, k), __builtin_bit_cast(bf16x2, q), acc, false);
}
__device__ __forceinline__ float dot2z(unsigned k, unsigned q) {
  float r; asm("v_dot2_f32_bf16 %0, %1, %2, 0" : "=v"(r) : "v"(k), "v"(q)); return r;
}
__device__ __forceinline__ v4u ldnt(const u16* p) { return __builtin_nontemporal_load((const v4u*)p); }
__device__ __forceinline__ v4u mkgran(float a, float b, unsigned A, unsigned B) {
  const unsigned v0 = __float_as_uint(a), v1 = __float_as_uint(b);
  v4u g; g[0] = v0; g[1] = v1; g[2] = v0 ^ A; g[3] = v1 ^ B;
  return g;
}
__device__ __forceinline__ unsigned xor3(unsigned a, unsigned b, unsigned c) {
  unsigned r; asm("v_bitop3_b32 %0, %1, %2, %3 bitop3:0x96" : "=v"(r) : "v"(a), "v"(b), "v"(c)); return r;
}
__device__ __forceinline__ unsigned or3(unsigned a, unsigned b, unsigned c) {
  unsigned r; asm("v_or3_b32 %0, %1, %2, %3" : "=v"(r) : "v"(a), "v"(b), "v"(c)); return r;
}
__device__ __forceinline__ unsigned badgran(unsigned bad, v4u g, unsigned A, unsigned B) {
  return or3(bad, xor3(g[0], g[2], A), xor3(g[1], g[3], B));
}
__device__ __forceinline__ unsigned pkbf(float lo, float hi) {
  unsigned r; asm("v_cvt_pk_bf16_f32 %0, %1, %2" : "=v"(r) : "v"(lo), "v"(hi)); return r;
}
__device__ __forceinline__ unsigned plo(unsigned a, unsigned b) { return __builtin_amdgcn_perm(b, a, 0x05040100u); }
__device__ __forceinline__ unsigned phi(unsigned a, unsigned b) { return __builtin_amdgcn_perm(b, a, 0x07060302u); }
template <int CTRL> __device__ __forceinline__ float dpp(float x) {
  return __builtin_bit_cast(float, __builtin_amdgcn_mov_dpp(__builtin_bit_cast(int, x), CTRL, 0xf, 0xf, true));
}
__device__ __forceinline__ float rowbc12(float x) { return dpp<0x15C>(x); }
constexpr int XOR1 = 0xB1, XOR2 = 0x4E, XOR7 = 0x141, XOR8 = 0x128;
__device__ __forceinline__ float tr32(float a, float b) {
  auto s = __builtin_amdgcn_permlane32_swap(__float_as_uint(a), __float_as_uint(b), false, false);
  return __uint_as_float(s[0]) + __uint_as_float(s[1]);
}
__device__ __forceinline__ float tr16(float a, float b) {
  auto s = __builtin_amdgcn_permlane16_swap(__float_as_uint(a), __float_as_uint(b), false, false);
  return __uint_as_float(s[0]) + __uint_as_float(s[1]);
}
__device__ __forceinline__ float tr8(float a, float b, bool up) {
  const float keep = up ? b : a, send = up ? a : b;
  return keep + dpp<XOR8>(send);
}
__device__ __forceinline__ float max32(float x) {
  auto s = __builtin_amdgcn_permlane32_swap(__float_as_uint(x), __float_as_uint(x), false, false);
  return fmaxf(__uint_as_float(s[0]), __uint_as_float(s[1]));
}
__device__ __forceinline__ float max16(float x) {
  auto s = __builtin_amdgcn_permlane16_swap(__float_as_uint(x), __float_as_uint(x), false, false);
  return fmaxf(__uint_as_float(s[0]), __uint_as_float(s[1]));
}
__device__ __forceinline__ void reduce24(float (&v)[24], int lane, float* dst  ) {
#pragma unroll
  for (int k = 0; k < 12; ++k) v[k] = tr32(v[k], v[k + 12]);
#pragma unroll
  for (int k = 0; k < 6; ++k) v[k] = tr16(v[k], v[k + 6]);
  const bool up = lane & 8;
#pragma unroll
  for (int k = 0; k < 3; ++k) v[k] = tr8(v[k], v[k + 3], up);
  const int base = 3 * (lane >> 3), j = lane & 7;
#pragma unroll
  for (int k = 0; k < 3; ++k) { const int idx = base + k; dst[(idx >> 3) * 64 + 8 * j + (idx & 7)] = v[k]; }
}
#define PIN() __builtin_amdgcn_sched_barrier(0)

template <bool HAS_KV>
__device__ __forceinline__ void stream_role(const u16* __restrict__ x, const u16* __restrict__ kc, const u16* __restrict__ vc,
    const u16* __restrict__ wq, const u16* __restrict__ wo, int h, int s, int wave, int lane,
    float (*red)[192], float (*red2)[80], const unsigned* qs, float* os, float (*wml)[2], const unsigned* tag, unsigned char* ws) {
  const int rp = lane >> 3, j = lane & 7;
  const int qrow = 80 * s + 8 * wave + rp;
  const unsigned xr = x[qrow];
  PIN();
  v4u WQ[3], KK[2][3], VV[2][3], WO[6];
#pragma unroll
  for (int i = 0; i < 3; ++i) WQ[i] = ldnt(wq + (size_t)qrow * (HQ * DH) + h * DH + 64 * i + 8 * j);
  const size_t kvbase = ((size_t)h * SK + (SK - W) + KPS * s + 16 * wave + rp) * DH + 8 * j;
  PIN();
  if constexpr (HAS_KV) {
    asm volatile("s_waitcnt vmcnt(1)" ::: "memory"); PIN();
#pragma unroll
    for (int g = 0; g < 2; ++g)
#pragma unroll
      for (int i = 0; i < 3; ++i) KK[g][i] = ldnt(kc + kvbase + (size_t)g * 8 * DH + 64 * i);
    PIN();
  }
  {
    const float xv = __uint_as_float(xr << 16);
    float a[24];
#pragma unroll
    for (int i = 0; i < 3; ++i)
#pragma unroll
      for (int e = 0; e < 4; ++e) { a[8 * i + 2 * e] = xv * bflo(WQ[i][e]); a[8 * i + 2 * e + 1] = xv * bfhi(WQ[i][e]); }
    reduce24(a, lane, red[wave]);
  }
  __syncthreads();
  const int r4 = lane >> 4, cc = lane & 15;
  __syncthreads();
  PIN();
  if constexpr (HAS_KV) {
#pragma unroll
    for (int g = 0; g < 2; ++g)
#pragma unroll
      for (int i = 0; i < 3; ++i) VV[g][i] = ldnt(vc + kvbase + (size_t)g * 8 * DH + 64 * i);
    PIN();
    asm volatile("s_waitcnt vmcnt(6)" ::: "memory"); PIN();
    __syncthreads();
    PIN();
#pragma unroll
    for (int i = 0; i < 3; ++i)
#pragma unroll
      for (int e = 0; e < 4; ++e) {
        const unsigned lo = plo(VV[0][i][e], VV[1][i][e]), hi = phi(VV[0][i][e], VV[1][i][e]);
        VV[0][i][e] = lo; VV[1][i][e] = hi;
      }
#pragma unroll
    for (int i = 0; i < 3; ++i) asm volatile("" : "+v"(VV[0][i]), "+v"(VV[1][i]));
    PIN();
  } else {
    __syncthreads();
  }
  __syncthreads();
  if constexpr (HAS_KV) {
    const u16* wb = wo + (size_t)(h * DH + 24 * wave + r4) * D + 80 * s + 8 * (cc < 10 ? cc : 9);
#pragma unroll
    for (int i = 0; i < 6; ++i) WO[i] = ldnt(wb + (size_t)(4 * i) * D);
    PIN();
    v4u qp[3];
#pragma unroll
    for (int i = 0; i < 3; ++i) qp[i] = *(const v4u*)&qs[32 * i + 4 * j];
    float sv[2];
#pragma unroll
    for (int g = 0; g < 2; ++g) {
      float dd[3];
#pragma unroll
      for (int i = 0; i < 3; ++i) {
        dd[i] = dot2z(KK[g][i][0], qp[i][0]);
#pragma unroll
        for (int e = 1; e < 4; ++e) dd[i] = dot2(KK[g][i][e], qp[i][e], dd[i]);
      }
      float d = (dd[0] + dd[1]) + dd[2];
      d += dpp<XOR1>(d); d += dpp<XOR2>(d); d += dpp<XOR7>(d);
      sv[g] = rbf(rbf(d) * 0.07216878364870322f);
    }
    float m = fmaxf(sv[0], sv[1]);
    m = fmaxf(m, dpp<XOR8>(m)); m = max16(m); m = max32(m);
    const float p0 = __expf(sv[0] - m), p1 = __expf(sv[1] - m);
    float l = p0 + p1;
    l += dpp<XOR8>(l); l = tr16(l, l); l = tr32(l, l);
    float a[24];
    const unsigned pp = pkbf(p0, p1);
#pragma unroll
    for (int i = 0; i < 3; ++i)
#pragma unroll
      for (int e = 0; e < 4; ++e) {
        a[8 * i + 2 * e] = dot2z(VV[0][i][e], pp);
        a[8 * i + 2 * e + 1] = dot2z(VV[1][i][e], pp);
      }
    reduce24(a, lane, red[wave]);
    if (lane == 0) { wml[wave][0] = m; wml[wave][1] = l; }
  }
  __syncthreads();
  PIN();
  if constexpr (HAS_KV) {
    const __amdgpu_buffer_rsrc_t rs = __builtin_amdgcn_make_buffer_rsrc(ws, 0, WS_END, 0x00020000);
    const unsigned A = tag[0], B = tag[1];
    const int gi = lane & 15;
    {
      float M = wml[0][0];
#pragma unroll
      for (int w = 1; w < 8; ++w) M = fmaxf(M, wml[w][0]);
      float s0 = 0.f, s1 = 0.f, L = 0.f;
#pragma unroll
      for (int w = 0; w < 8; ++w) {
        const float c = __expf(wml[w][0] - M);
        L += c * wml[w][1];
        const float2 u = *(const float2*)&red[w][24 * wave + 2 * (gi < 12 ? gi : 0)];
        s0 += c * u.x; s1 += c * u.y;
      }
      if (gi == 12) { s0 = M; s1 = L; }
      if (lane < 13)
        __builtin_amdgcn_raw_buffer_store_b128(mkgran(s0, s1, A, B), rs, WS_OX + (unsigned)((h * 8 + wave) * 8 + s) * 256u + lane * 16, 0, 16);
    }
#pragma unroll
    for (int i = 0; i < 6; i += 2)
#pragma unroll
      for (int e = 0; e < 4; ++e) {
        const unsigned lo = plo(WO[i][e], WO[i + 1][e]), hi = phi(WO[i][e], WO[i + 1][e]);
        WO[i][e] = lo; WO[i + 1][e] = hi;
      }
#pragma unroll
    for (int i = 0; i < 6; ++i) asm volatile("" : "+v"(WO[i]));
    PIN();
    v4u g[2];
    const unsigned off = WS_OX + (unsigned)((h * 8 + wave) * 8) * 256u + (gi < 13 ? lane : (lane & 48)) * 16;
    for (unsigned spin = 0; spin < SPIN_CAP; ++spin) {
      asm volatile("" ::: "memory");
      g[0] = __builtin_amdgcn_raw_buffer_load_b128(rs, off, 0, 16);
      g[1] = __builtin_amdgcn_raw_buffer_load_b128(rs, off + 1024, 0, 16);
      const unsigned bad = badgran(badgran(0u, g[0], A, B), g[1], A, B);
      if (__all(bad == 0)) break;
      __builtin_amdgcn_s_sleep(1);
    }
    const float m0 = rowbc12(__uint_as_float(g[0][0])), l0 = rowbc12(__uint_as_float(g[0][1]));
    const float m1 = rowbc12(__uint_as_float(g[1][0])), l1 = rowbc12(__uint_as_float(g[1][1]));
    const float M = max32(max16(fmaxf(m0, m1)));
    const float c0 = __expf(m0 - M), c1 = __expf(m1 - M);
    float L = c0 * l0 + c1 * l1;
    float o0 = c0 * __uint_as_float(g[0][0]) + c1 * __uint_as_float(g[1][0]);
    float o1 = c0 * __uint_as_float(g[0][1]) + c1 * __uint_as_float(g[1][1]);
    L = tr16(L, L); L = tr32(L, L);
    o0 = tr16(o0, o0); o0 = tr32(o0, o0);
    o1 = tr16(o1, o1); o1 = tr32(o1, o1);
    const float inv = 1.0f / L;
    if (lane < 12) *(float2*)&os[24 * wave + 2 * lane] = make_float2(rbf(o0 * inv), rbf(o1 * inv));
    __builtin_amdgcn_wave_barrier();
    asm volatile("s_waitcnt lgkmcnt(0)" ::: "memory");
    float a[8];
#pragma unroll
    for (int e = 0; e < 8; ++e) a[e] = 0.f;
#pragma unroll
    for (int i = 0; i < 6; i += 2) {
      const unsigned op = __float_as_uint(os[24 * wave + 4 * i + r4]) >> 16 | __float_as_uint(os[24 * wave + 4 * i + 4 + r4]);
#pragma unroll
      for (int e = 0; e < 4; ++e) {
        a[2 * e] = dot2(WO[i][e], op, a[2 * e]);
        a[2 * e + 1] = dot2(WO[i + 1][e], op, a[2 * e + 1]);
      }
    }
#pragma unroll
    for (int k = 0; k < 4; ++k) a[k] = tr32(a[k], a[k + 4]);
#pragma unroll
    for (int k = 0; k < 2; ++k) a[k] = tr16(a[k], a[k + 2]);
    if (cc < 10) *(float2*)&red2[wave][8 * cc + 2 * (r4 & 1) + 4 * (r4 >> 1)] = make_float2(a[0], a[1]);
  }
  __syncthreads();
}

__global__ void __launch_bounds__(NT, 1)
fused_decode(const u16* __restrict__ x, const u16* __restrict__ kc, const u16* __restrict__ vc,
             const u16* __restrict__ wq, const u16* __restrict__ wo, u16* __restrict__ out, unsigned char* ws) {
  __shared__ __attribute__((aligned(16))) float red[NWS][192];
  __shared__ __attribute__((aligned(16))) float red2[8][80];
  __shared__ __attribute__((aligned(16))) unsigned qs[96];
  __shared__ __attribute__((aligned(16))) float os[192];
  __shared__ __attribute__((aligned(16))) float wml[8][2];
  __shared__ unsigned tag[2];

  const int b = blockIdx.x;
  const int h = (b & 7) * 4 + ((b >> 3) & 3), s = b >> 5;
  const int tid = threadIdx.x, lane = tid & 63;
  const int wave = __builtin_amdgcn_readfirstlane(tid >> 6);

  if (wave >= 1 && wave <= 8) {
    stream_role<true>(x, kc, vc, wq, wo, h, s, wave - 1, lane, red, red2, qs, os, wml, tag, ws);
  } else if (wave > 8) {
    stream_role<false>(x, kc, vc, wq, wo, h, s, wave - 1, lane, red, red2, qs, os, wml, tag, ws);
  } else {
    const __amdgpu_buffer_rsrc_t rs = __builtin_amdgcn_make_buffer_rsrc(ws, 0, WS_END, 0x00020000);
    const unsigned long long E = __hip_atomic_load((unsigned long long*)(ws + WS_E), __ATOMIC_RELAXED, __HIP_MEMORY_SCOPE_AGENT);
    const unsigned long long T = E + 1ull;
    const unsigned A = 2u * (unsigned)T + 1u, B = (unsigned)(T >> 31) ^ ((unsigned)T * 0x9E3779B9u);
    if (lane == 0) { tag[0] = A; tag[1] = B; }
    __syncthreads();
    {
      float s0 = 0.f, s1 = 0.f, t0 = 0.f, t1 = 0.f;
#pragma unroll
      for (int w = 0; w < NWS; ++w) {
        const float2 u = *(const float2*)&red[w][2 * lane];
        s0 += u.x; s1 += u.y;
        const float2 v = *(const float2*)&red[w][128 + 2 * (lane & 31)];
        t0 += v.x; t1 += v.y;
      }
      const unsigned off = WS_QX + (unsigned)(h * 8 + s) * 2048u;
      __builtin_amdgcn_raw_buffer_store_b128(mkgran(s0, s1, A, B), rs, off + lane * 16, 0, 16);
      if (lane < 32) __builtin_amdgcn_raw_buffer_store_b128(mkgran(t0, t1, A, B), rs, off + 1024 + lane * 16, 0, 16);
    }
    PIN();
    __syncthreads();
    __syncthreads();
    {
      v4u g0[8], g1[8];
      const unsigned base = WS_QX + (unsigned)h * 16384u;
      for (unsigned spin = 0; spin < SPIN_CAP; ++spin) {
        asm volatile("" ::: "memory");
        unsigned bad = 0;
#pragma unroll
        for (int p = 0; p < 8; ++p) {
          g0[p] = __builtin_amdgcn_raw_buffer_load_b128(rs, base + p * 2048 + lane * 16, 0, 16);
          g1[p] = __builtin_amdgcn_raw_buffer_load_b128(rs, base + p * 2048 + 1024 + (lane & 31) * 16, 0, 16);
        }
#pragma unroll
        for (int p = 0; p < 8; ++p) bad = badgran(badgran(bad, g0[p], A, B), g1[p], A, B);
        if (__all(bad == 0)) break;
        __builtin_amdgcn_s_sleep(1);
      }
      float s0 = 0.f, s1 = 0.f, t0 = 0.f, t1 = 0.f;
#pragma unroll
      for (int p = 0; p < 8; ++p) {
        s0 += __uint_as_float(g0[p][0]); s1 += __uint_as_float(g0[p][1]);
        t0 += __uint_as_float(g1[p][0]); t1 += __uint_as_float(g1[p][1]);
      }
      qs[lane] = pkbf(s0, s1);
      if (lane < 32) qs[64 + lane] = pkbf(t0, t1);
    }
    __syncthreads();
    __syncthreads();
    __syncthreads();
    if (lane < 40) {
      float o0 = 0.f, o1 = 0.f;
#pragma unroll
      for (int w = 0; w < 8; ++w) { const float2 u = *(const float2*)&red2[w][2 * lane]; o0 += u.x; o1 += u.y; }
      __builtin_amdgcn_raw_buffer_store_b128(mkgran(o0, o1, A, B), rs,
          WS_PX + (unsigned)(s * 5 + (lane >> 3)) * 4096u + (unsigned)h * 128u + (lane & 7) * 16, 0, 16);
    }
    if (b < NRED) {
      v4u g[4];
      const unsigned off = WS_PX + (unsigned)b * 4096u + (unsigned)lane * 16u;
      for (unsigned spin = 0; spin < SPIN_CAP; ++spin) {
        asm volatile("" ::: "memory");
        unsigned bad = 0;
#pragma unroll
        for (int k = 0; k < 4; ++k) g[k] = __builtin_amdgcn_raw_buffer_load_b128(rs, off + k * 1024, 0, 16);
#pragma unroll
        for (int k = 0; k < 4; ++k) bad = badgran(bad, g[k], A, B);
        if (__all(bad == 0)) break;
        __builtin_amdgcn_s_sleep(1);
      }
      float v0 = (__uint_as_float(g[0][0]) + __uint_as_float(g[1][0])) + (__uint_as_float(g[2][0]) + __uint_as_float(g[3][0]));
      float v1 = (__uint_as_float(g[0][1]) + __uint_as_float(g[1][1])) + (__uint_as_float(g[2][1]) + __uint_as_float(g[3][1]));
      v0 = tr32(v0, v1);
      v0 = tr16(v0, v0);
      v0 += dpp<XOR8>(v0);
      if ((lane & 24) == 0) out[b * 16 + 2 * (lane & 7) + (lane >> 5)] = (u16)(__float_as_uint(rbf(v0)) >> 16);
      if (b == 0 && lane == 8) {
        __hip_atomic_store((unsigned long long*)(ws + WS_E), T, __ATOMIC_RELAXED, __HIP_MEMORY_SCOPE_AGENT);
      }
    }
  }
}

extern "C" void kernel_launch(void* const* d_in, const int* in_sizes, int n_in,
                              void* d_out, int out_size, void* d_ws, size_t ws_size,
                              hipStream_t stream) {
  fused_decode<<<256, NT, 0, stream>>>((const u16*)d_in[0], (const u16*)d_in[1], (const u16*)d_in[2],
                                       (const u16*)d_in[3], (const u16*)d_in[4], (u16*)d_out, (unsigned char*)d_ws);
}
```
